```python
import torch

def _softmax(x, axis=-1):
    return torch.softmax(x, dim=axis)

def _gelu(x):
    return torch.nn.functional.gelu(x, approximate='tanh')

def _silu(x):
    return torch.nn.functional.silu(x)

def _sigmoid(x):
    return torch.sigmoid(x)

def _layernorm(x, axis=-1, eps=1e-05):
    m = x.mean(dim=axis, keepdim=True)
    v = x.var(dim=axis, keepdim=True, unbiased=False)
    return (x - m) / torch.sqrt(v + eps)

def _scatter_add(tgt, idx, src, axis=0):
    return tgt.clone().index_add_(axis, idx, src)

def _segment_sum(src, seg, n):
    out = torch.zeros((n,) + src.shape[1:], dtype=src.dtype)
    return out.index_add_(0, seg, src)

def _pad(x, pw):
    flat = tuple((v for pair in reversed(pw) for v in pair))
    return torch.nn.functional.pad(x, flat)
kernel_name = 'sequential_preLN__memory_aug_recurrent_mem_tr__dense_activation_gelu__prompt'

def setup_inputs():

    _w = lambda a: (lambda t: t.to(torch.bfloat16) if t.is_floating_point() else t)(torch.as_tensor(a))

    def _A_setup():
        B, S, Dm = (1, 2048, 576)
        H, Dh = (32, 128)
        M, seg = (8, 32)
        seg = min(seg, S)
        S = S // seg * seg
        return {'x': _w((lambda *s: torch.randn(*s, dtype=torch.bfloat16) * 0.05)(B, S, Dm)), 'mem0': _w((lambda *s: torch.randn(*s, dtype=torch.bfloat16) * 0.2)(B, M, Dm)), 'Wq': _w((lambda *s: torch.randn(*s, dtype=torch.bfloat16) * 1.6)(Dm, H, Dh) * Dm ** (-0.5)), 'Wk': _w((lambda *s: torch.randn(*s, dtype=torch.bfloat16) * 1.6)(Dm, H, Dh) * Dm ** (-0.5)), 'Wv': _w((lambda *s: torch.randn(*s, dtype=torch.bfloat16) * 0.2)(Dm, H, Dh) * Dm ** (-0.5)), 'Wo': _w((lambda *s: torch.randn(*s, dtype=torch.bfloat16) * 0.2)(H, Dh, Dm) * (H * Dh) ** (-0.5)), 'seg_len': seg}
    _A_in = _A_setup()

    def _B_setup():
        B, S, D, F = (1, 2048, 576, 4608)
        return {'x': _w((lambda *s: torch.randn(*s, dtype=torch.bfloat16) * 0.05)(B, S, D)), 'w1': _w((lambda *s: torch.randn(*s, dtype=torch.bfloat16) * 0.4)(D, F) * D ** (-0.5)), 'b1': _w((lambda *s: torch.randn(*s, dtype=torch.bfloat16) * 12.8)(F) * 0.01), 'w2': _w((lambda *s: torch.randn(*s, dtype=torch.bfloat16) * 0.2)(F, D) * F ** (-0.5)), 'b2': _w((lambda *s: torch.randn(*s, dtype=torch.bfloat16) * 3.2)(D) * 0.01)}
    _B_in = _B_setup()
    return {'A_x': _A_in['x'], 'A_mem0': _A_in['mem0'], 'A_Wq': _A_in['Wq'], 'A_Wk': _A_in['Wk'], 'A_Wv': _A_in['Wv'], 'A_Wo': _A_in['Wo'], 'A_seg_len': _A_in['seg_len'], 'B_x': _B_in['x'], 'B_w1': _B_in['w1'], 'B_b1': _B_in['b1'], 'B_w2': _B_in['w2'], 'B_b2': _B_in['b2']}

def reference(A_x, A_mem0, A_Wq, A_Wk, A_Wv, A_Wo, A_seg_len, B_x, B_w1, B_b1, B_w2, B_b2):

    def _A_ref(x, mem0, Wq, Wk, Wv, Wo, seg_len):
        B, S, Dm = x.shape
        H, Dh = (Wq.shape[1], Wq.shape[2])
        M = mem0.shape[1]
        scale = Dh ** (-0.5)
        n_seg = S // seg_len
        L = M + seg_len
        idx = torch.arange(L)
        mask = (idx[:, None] < M) | (idx[:, None] >= idx[None, :])
        mem = mem0
        outs = []
        for s in range(n_seg):
            xs = x[:, s * seg_len:(s + 1) * seg_len, :].clone()
            h = torch.concatenate([mem, xs], axis=1)
            q = torch.einsum('btd,dhe->bhte', h, Wq)
            k = torch.einsum('btd,dhe->bhte', h, Wk)
            v = torch.einsum('btd,dhe->bhte', h, Wv)
            scores = torch.einsum('bhqe,bhke->bhqk', q, k) * scale
            scores = torch.where(mask[None, None], scores, -1000000000.0)
            p = _softmax(scores, axis=-1)
            o = torch.einsum('bhqk,bhke->bhqe', p, v)
            o = torch.einsum('bhte,hed->btd', o, Wo)
            mem = o[:, :M, :].clone()
            outs.append(o[:, M:, :])
        return torch.concatenate(outs, axis=1)

    def _B_ref(x, w1, b1, w2, b2):
        h = torch.einsum('bsd,df->bsf', x, w1) + b1
        h = _gelu(h)
        return torch.einsum('bsf,fd->bsd', h, w2) + b2
    x = A_x
    h = x + _A_ref(_layernorm(x, axis=-1), A_mem0, A_Wq, A_Wk, A_Wv, A_Wo, A_seg_len)
    h2 = h + _B_ref(_layernorm(h, axis=-1), B_w1, B_b1, B_w2, B_b2)
    return h2
```

```cpp
#include <hip/hip_runtime.h>
#include <hip/hip_bf16.h>
#include <stdint.h>
typedef unsigned short u16;
typedef __bf16 bf16x8 __attribute__((ext_vector_type(8)));
typedef float f32x4 __attribute__((ext_vector_type(4)));
typedef unsigned v4u __attribute__((ext_vector_type(4)));

constexpr int S_ = 2048, D_ = 576, H_ = 32, E_ = 128, M_ = 8, SEG = 32, NSEG = 64, F_ = 4608, HE = 4096;

__device__ __forceinline__ float bf2f(u16 v) { return __uint_as_float((unsigned)v << 16); }
__device__ __forceinline__ u16 f2bf(float f) { __bf16 b = (__bf16)f; return __builtin_bit_cast(u16, b); }
__device__ __forceinline__ float rbf(float f) { return bf2f(f2bf(f)); }

__global__ void k_ln(const u16* __restrict__ x, u16* __restrict__ y, int rows) {
  int row = blockIdx.x * 4 + (threadIdx.x >> 6), lane = threadIdx.x & 63;
  if (row >= rows) return;
  float v[9]; float s = 0;
  for (int i = 0; i < 9; ++i) { v[i] = bf2f(x[row * D_ + i * 64 + lane]); s += v[i]; }
  for (int o = 32; o; o >>= 1) s += __shfl_xor(s, o);
  float mean = s * (1.f / D_); float q = 0;
  for (int i = 0; i < 9; ++i) { float d = v[i] - mean; q += d * d; }
  for (int o = 32; o; o >>= 1) q += __shfl_xor(q, o);
  float var = rbf(q * (1.f / D_)); float mb = rbf(mean);
  float den = rbf(sqrtf(rbf(var + 1e-5f)));
  for (int i = 0; i < 9; ++i) y[row * D_ + i * 64 + lane] = f2bf(rbf(v[i] - mb) / den);
}

template <int EPI>
__global__ void __launch_bounds__(256) k_gemm(const u16* __restrict__ A, int lda, const u16* __restrict__ B, int ldb, u16* __restrict__ C, int ldc,
                       int Mrows, int K, const u16* __restrict__ bias, const u16* __restrict__ res) {
  __shared__ __attribute__((aligned(16))) u16 As[64][40];
  __shared__ __attribute__((aligned(16))) u16 Bs[64][40];
  int tid = threadIdx.x, lane = tid & 63, w = tid >> 6, wr = w >> 1, wc = w & 1;
  int m0 = blockIdx.y * 64, n0 = blockIdx.x * 64;
  f32x4 acc[2][2] = {};
  for (int k0 = 0; k0 < K; k0 += 32) {
    { int r = tid >> 2, c = (tid & 3) * 8; v4u v = {0, 0, 0, 0};
      if (m0 + r < Mrows) v = *(const v4u*)(A + (size_t)(m0 + r) * lda + k0 + c);
      *(v4u*)&As[r][c] = v; }
    { int kk = tid >> 3, c = (tid & 7) * 8; v4u v = *(const v4u*)(B + (size_t)(k0 + kk) * ldb + n0 + c);
      const u16* p = (const u16*)&v; for (int j = 0; j < 8; ++j) Bs[c + j][kk] = p[j]; }
    __syncthreads();
    bf16x8 a[2], b[2];
    for (int i = 0; i < 2; ++i) { a[i] = *(const bf16x8*)&As[wr * 32 + i * 16 + (lane & 15)][(lane >> 4) * 8]; b[i] = *(const bf16x8*)&Bs[wc * 32 + i * 16 + (lane & 15)][(lane >> 4) * 8]; }
    for (int i = 0; i < 2; ++i) for (int j = 0; j < 2; ++j) acc[i][j] = __builtin_amdgcn_mfma_f32_16x16x32_bf16(a[i], b[j], acc[i][j], 0, 0, 0);
    __syncthreads();
  }
  for (int i = 0; i < 2; ++i) for (int j = 0; j < 2; ++j) for (int r = 0; r < 4; ++r) {
    int m = m0 + wr * 32 + i * 16 + (lane >> 4) * 4 + r, n = n0 + wc * 32 + j * 16 + (lane & 15);
    if (m >= Mrows) continue;
    float v = rbf(acc[i][j][r]);
    if (EPI == 1) v = bf2f(res[(size_t)m * ldc + n]) + v;
    if (EPI == 2) { v = rbf(v + bf2f(bias[n])); float u = 0.7978845608028654f * (v + 0.044715f * v * v * v); v = 0.5f * v * (1.f + tanhf(u)); }
    if (EPI == 3) { v = rbf(v + bf2f(bias[n])); v = bf2f(res[(size_t)m * ldc + n]) + v; }
    C[(size_t)m * ldc + n] = f2bf(v);
  }
}

template <bool MEMQ>
__global__ void __launch_bounds__(256) k_attn(const u16* __restrict__ Q, const u16* __restrict__ Kx, const u16* __restrict__ Vx, const u16* __restrict__ Km, const u16* __restrict__ Vm,
                       u16* __restrict__ O, int seg0) {
  __shared__ float qs[32][129], ks[40][129], vs[40][128], ps[32][41];
  int h = blockIdx.x, seg = seg0 + blockIdx.y, tid = threadIdx.x;
  constexpr int NQ = MEMQ ? 8 : 32;
  const u16* q = Q + (size_t)seg * NQ * HE + h * E_;
  for (int i = tid; i < NQ * 128; i += 256) qs[i >> 7][i & 127] = bf2f(q[(size_t)(i >> 7) * HE + (i & 127)]);
  for (int i = tid; i < 40 * 128; i += 256) { int r = i >> 7, c = i & 127;
    size_t off = r < 8 ? ((size_t)(seg * 8 + r) * HE + h * E_ + c) : ((size_t)(seg * 32 + r - 8) * HE + h * E_ + c);
    ks[r][c] = bf2f(r < 8 ? Km[off] : Kx[off]); vs[r][c] = bf2f(r < 8 ? Vm[off] : Vx[off]); }
  __syncthreads();
  for (int i = tid; i < NQ * 40; i += 256) { int r = i / 40, c = i % 40; float s = 0; for (int e = 0; e < 128; ++e) s += qs[r][e] * ks[c][e];
    s = rbf(rbf(s) * 0.08838834764831845f); if (!MEMQ && c > r + 8) s = -1e9f; ps[r][c] = s; }
  __syncthreads();
  if (tid < NQ) { float m = -3e38f; for (int c = 0; c < 40; ++c) m = fmaxf(m, ps[tid][c]); float s = 0; for (int c = 0; c < 40; ++c) { float e = __expf(ps[tid][c] - m); ps[tid][c] = e; s += e; }
    for (int c = 0; c < 40; ++c) ps[tid][c] = rbf(ps[tid][c] / s); }
  __syncthreads();
  u16* o = O + (size_t)seg * NQ * HE + h * E_;
  for (int i = tid; i < NQ * 128; i += 256) { int r = i >> 7, e = i & 127; float s = 0; for (int c = 0; c < 40; ++c) s += ps[r][c] * vs[c][e]; o[(size_t)r * HE + e] = f2bf(s); }
}

extern "C" void kernel_launch(void* const* d_in, const int* in_sizes, int n_in, void* d_out, int out_size, void* d_ws, size_t ws_size, hipStream_t stream) {
  const u16* x = (const u16*)d_in[0]; const u16* mem0 = (const u16*)d_in[1];
  const u16 *Wq = (const u16*)d_in[2], *Wk = (const u16*)d_in[3], *Wv = (const u16*)d_in[4], *Wo = (const u16*)d_in[5];
  const u16 *w1 = (const u16*)d_in[8], *b1 = (const u16*)d_in[9], *w2 = (const u16*)d_in[10], *b2 = (const u16*)d_in[11];
  u16* out = (u16*)d_out; u16* ws = (u16*)d_ws;
  size_t o = 0; auto alloc = [&](size_t n) { u16* p = ws + o; o += (n + 127) / 128 * 128; return p; };
  u16* mem = alloc(65 * 8 * D_);
  u16* xn = alloc(S_ * D_); u16* Q = alloc((size_t)S_ * HE); u16* K = alloc((size_t)S_ * HE); u16* V = alloc((size_t)S_ * HE); u16* O = alloc((size_t)S_ * HE);
  u16* Qm = alloc(512 * HE); u16* Km = alloc(512 * HE); u16* Vm = alloc(512 * HE); u16* Om = alloc(512 * HE);
  u16* h = alloc(S_ * D_); u16* hn = alloc(S_ * D_); u16* hid = alloc((size_t)S_ * F_);
  k_ln<<<S_ / 4, 256, 0, stream>>>(x, xn, S_);
  k_gemm<0><<<dim3(HE / 64, S_ / 64), 256, 0, stream>>>(xn, D_, Wq, HE, Q, HE, S_, D_, nullptr, nullptr);
  k_gemm<0><<<dim3(HE / 64, S_ / 64), 256, 0, stream>>>(xn, D_, Wk, HE, K, HE, S_, D_, nullptr, nullptr);
  k_gemm<0><<<dim3(HE / 64, S_ / 64), 256, 0, stream>>>(xn, D_, Wv, HE, V, HE, S_, D_, nullptr, nullptr);
  hipMemcpyAsync(mem, mem0, 8 * D_ * 2, hipMemcpyDeviceToDevice, stream);
  for (int s = 0; s < NSEG; ++s) {
    u16* m = mem + s * 8 * D_;
    k_gemm<0><<<dim3(HE / 64, 1), 256, 0, stream>>>(m, D_, Wk, HE, Km + (size_t)s * 8 * HE, HE, 8, D_, nullptr, nullptr);
    k_gemm<0><<<dim3(HE / 64, 1), 256, 0, stream>>>(m, D_, Wv, HE, Vm + (size_t)s * 8 * HE, HE, 8, D_, nullptr, nullptr);
    if (s + 1 < NSEG) {
      k_gemm<0><<<dim3(HE / 64, 1), 256, 0, stream>>>(m, D_, Wq, HE, Qm + (size_t)s * 8 * HE, HE, 8, D_, nullptr, nullptr);
      k_attn<true><<<dim3(H_, 1), 256, 0, stream>>>(Qm, K, V, Km, Vm, Om, s);
      k_gemm<0><<<dim3(D_ / 64, 1), 256, 0, stream>>>(Om + (size_t)s * 8 * HE, HE, Wo, D_, m + 8 * D_, D_, 8, HE, nullptr, nullptr);
    }
  }
  k_attn<false><<<dim3(H_, NSEG), 256, 0, stream>>>(Q, K, V, Km, Vm, O, 0);
  k_gemm<1><<<dim3(D_ / 64, S_ / 64), 256, 0, stream>>>(O, HE, Wo, D_, h, D_, S_, HE, nullptr, x);
  k_ln<<<S_ / 4, 256, 0, stream>>>(h, hn, S_);
  k_gemm<2><<<dim3(F_ / 64, S_ / 64), 256, 0, stream>>>(hn, D_, w1, F_, hid, F_, S_, D_, b1, nullptr);
  k_gemm<3><<<dim3(D_ / 64, S_ / 64), 256, 0, stream>>>(hid, F_, w2, D_, out, D_, S_, F_, b2, h);
}
```

```cpp
#include <hip/hip_runtime.h>
#include <stdint.h>
#include <utility>
typedef unsigned short u16;
typedef __bf16 bf16x8 __attribute__((ext_vector_type(8)));
typedef short s16x4 __attribute__((ext_vector_type(4)));
typedef float f32x4 __attribute__((ext_vector_type(4)));
typedef float f32x2 __attribute__((ext_vector_type(2)));
typedef unsigned v4u __attribute__((ext_vector_type(4)));
typedef unsigned v2u __attribute__((ext_vector_type(2)));
typedef unsigned v16u __attribute__((ext_vector_type(16)));
typedef unsigned v32u __attribute__((ext_vector_type(32)));
typedef __bf16 bf16x2 __attribute__((ext_vector_type(2)));
typedef __amdgpu_buffer_rsrc_t rsrc_t;
#define A_ASM_TEXT \
  "s_mov_b32 s56, 0\n\t" \
  "s_mov_b32 s57, s48\n\t" \
  "s_mov_b32 s81, s48\n\t" \
  "s_mov_b32 s64, 0\n\t" \
  "s_mov_b32 s80, 1568\n\t" \
  "s_mov_b32 s82, 8\n\t" \
  "s_mov_b32 s59, 118\n\t" \
  "s_mov_b32 s76, 0x3e0293ee\n\t" \
  "s_mov_b32 s77, 0x3e0293ee\n\t" \
  "s_mov_b32 s90, 0\n\t" \
  "s_mov_b32 s91, 0\n\t" \
  "s_mov_b32 s92, 0\n\t" \
  "s_mov_b32 s93, 0\n\t" \
  "s_mov_b32 s94, 0\n\t" \
  "s_mov_b32 s95, 0\n\t" \
  "s_mov_b32 s96, 0\n\t" \
  "s_mov_b32 s97, 0\n\t" \
  "s_mov_b32 s98, 0\n\t" \
  "s_mov_b32 s99, 0\n\t" \
  "v_mov_b32 v16, 0x3f803f80\n\t" \
  "v_mov_b32 v17, 0x3f803f80\n\t" \
  "v_mov_b32 v18, 0x3f803f80\n\t" \
  "v_mov_b32 v19, 0x3f803f80\n\t" \
  "v_mov_b32 v21, 0\n\t" \
  "v_mov_b32 v22, 0\n\t" \
  "s_and_b32 s60, s56, 3\n\t" \
  "s_mul_i32 s61, s60, 180224\n\t" \
  "s_add_u32 s68, s41, s61\n\t" \
  "s_add_u32 s69, s42, s61\n\t" \
  "s_add_u32 s70, s40, s61\n\t" \
  "s_add_u32 s72, s46, s61\n\t" \
  "s_xor_b32 s60, s60, 2\n\t" \
  "s_mul_i32 s61, s60, 180224\n\t" \
  "s_add_u32 s71, s40, s61\n\t" \
  "s_lshl_b32 s60, s56, 18\n\t" \
  "s_add_u32 s78, s44, s60\n\t" \
  "s_add_u32 s79, s45, s60\n\t" \
  "s_add_u32 s60, s56, 0\n\t" \
  "s_min_u32 s61, s60, 62\n\t" \
  "s_lshl_b32 s61, s61, 19\n\t" \
  "s_add_u32 s73, s43, s61\n\t" \
  "s_and_b32 s60, s60, 1\n\t" \
  "s_lshl_b32 s60, s60, 14\n\t" \
  "s_add_u32 m0, s60, s54\n\t" \
  "s_nop 0\n\t" \
  "buffer_load_dwordx4 v0, s[24:27], s73 offen offset:0 sc1 lds\n\t" \
  "buffer_load_dwordx4 v0, s[24:27], s73 offen offset:1024 sc1 lds\n\t" \
  "buffer_load_dwordx4 v0, s[24:27], s73 offen offset:2048 sc1 lds\n\t" \
  "buffer_load_dwordx4 v0, s[24:27], s73 offen offset:3072 sc1 lds\n\t" \
  "s_add_u32 s60, s56, 1\n\t" \
  "s_min_u32 s61, s60, 62\n\t" \
  "s_lshl_b32 s61, s61, 19\n\t" \
  "s_add_u32 s73, s43, s61\n\t" \
  "s_and_b32 s60, s60, 1\n\t" \
  "s_lshl_b32 s60, s60, 14\n\t" \
  "s_add_u32 m0, s60, s54\n\t" \
  "s_nop 0\n\t" \
  "buffer_load_dwordx4 v0, s[24:27], s73 offen offset:0 sc1 lds\n\t" \
  "buffer_load_dwordx4 v0, s[24:27], s73 offen offset:1024 sc1 lds\n\t" \
  "buffer_load_dwordx4 v0, s[24:27], s73 offen offset:2048 sc1 lds\n\t" \
  "buffer_load_dwordx4 v0, s[24:27], s73 offen offset:3072 sc1 lds\n\t" \
  "buffer_load_dwordx2 v[252:253], off, s[24:27], s47 sc1\n\t" \
  "ds_read_b128 v[164:167], v5 offset:0\n\t" \
  "v_mov_b64 v[100:101], 0\n\t" \
  "v_mov_b64 v[102:103], 0\n\t" \
  "ds_read_b128 v[168:171], v5 offset:1024\n\t" \
  "v_mov_b64 v[116:117], 0\n\t" \
  "ds_read_b128 v[172:175], v5 offset:2048\n\t" \
  "v_mov_b64 v[104:105], 0\n\t" \
  "v_mov_b64 v[106:107], 0\n\t" \
  "ds_read_b128 v[176:179], v5 offset:3072\n\t" \
  "v_mov_b64 v[118:119], 0\n\t" \
  "ds_read_b128 v[180:183], v5 offset:4096\n\t" \
  "v_mov_b64 v[108:109], 0\n\t" \
  "v_mov_b64 v[110:111], 0\n\t" \
  "ds_read_b128 v[184:187], v5 offset:5120\n\t" \
  "v_mov_b64 v[120:121], 0\n\t" \
  "ds_read_b128 v[188:191], v5 offset:6144\n\t" \
  "v_mov_b64 v[112:113], 0\n\t" \
  "v_mov_b64 v[114:115], 0\n\t" \
  "ds_read_b128 v[192:195], v5 offset:7168\n\t" \
  "v_mov_b64 v[122:123], 0\n\t" \
  "s_waitcnt vmcnt(5)\n\t" \
  "s_barrier\n\t" \
  "s_and_b32 s60, s56, 1\n\t" \
  "s_lshl_b32 s60, s60, 14\n\t" \
  "v_add_u32 v254, s60, v6\n\t" \
  "v_add_u32 v30, s60, v7\n\t" \
  "ds_read_b128 v[132:135], v254 offset:7232\n\t" \
  "ds_read_b128 v[136:139], v254 offset:8256\n\t" \
  "ds_read_b128 v[140:143], v254 offset:9280\n\t" \
  "ds_read_b128 v[144:147], v254 offset:10304\n\t" \
  "ds_read_b128 v[148:151], v254 offset:11328\n\t" \
  "ds_read_b128 v[152:155], v254 offset:12352\n\t" \
  "ds_read_b128 v[156:159], v254 offset:13376\n\t" \
  "ds_read_b128 v[160:163], v254 offset:14400\n\t" \
  "ds_read_b128 v[124:127], v30 offset:23616\n\t" \
  "ds_read_b128 v[128:131], v30 offset:24640\n\t" \
  "A_LOOP_%=:\n\t" \
  "s_mov_b32 s63, 0\n\t" \
  "s_mov_b32 s58, s57\n\t" \
  "A_TK_%=:\n\t" \
  "s_memrealtime s[66:67]\n\t" \
  "s_waitcnt lgkmcnt(0)\n\t" \
  "s_sub_u32 s91, s57, s66\n\t" \
  "s_cmp_gt_i32 s91, 3\n\t" \
  "s_cbranch_scc1 A_TK_%=\n\t" \
  "s_cmp_lt_i32 s91, 1\n\t" \
  "s_cbranch_scc1 A_TK0_%=\n\t" \
  "s_nop 3\n\t" \
  "s_cmp_lt_i32 s91, 2\n\t" \
  "s_cbranch_scc1 A_TKG_%=\n\t" \
  "s_nop 3\n\t" \
  "s_cmp_lt_i32 s91, 3\n\t" \
  "s_cbranch_scc1 A_TKG_%=\n\t" \
  "s_nop 4\n\t" \
  "s_branch A_TKG_%=\n\t" \
  "A_TK0_%=:\n\t" \
  "s_sub_u32 s58, s57, s91\n\t" \
  "s_cmp_lt_i32 s91, -4\n\t" \
  "s_cbranch_scc1 A_SLOW_%=\n\t" \
  "A_TKG_%=:\n\t" \
  "A_L2_%=:\n\t" \
  "buffer_load_dwordx4 v[64:67], v0, s[24:27], s68 offen sc1\n\t" \
  "s_nop 7\n\t" \
  "buffer_load_dwordx4 v[68:71], v0, s[24:27], s68 offen offset:1024 sc1\n\t" \
  "s_nop 7\n\t" \
  "buffer_load_dword v24, v12, s[24:27], s69 offen sc1\n\t" \
  "s_waitcnt vmcnt(2)\n\t" \
  "ds_write_b128 v7, v[64:67]\n\t" \
  "v_smfmac_f32_16x16x64_bf16 v[100:103], v[64:67], a[0:7], v1\n\t" \
  "v_max_u32 v30, v64, v66\n\t" \
  "v_smfmac_f32_16x16x64_bf16 v[104:107], v[64:67], a[8:15], v1\n\t" \
  "v_smfmac_f32_16x16x64_bf16 v[108:111], v[64:67], a[16:23], v1\n\t" \
  "v_smfmac_f32_16x16x64_bf16 v[112:115], v[64:67], a[24:31], v1\n\t" \
  "s_waitcnt vmcnt(1)\n\t" \
  "ds_write_b128 v7, v[68:71] offset:1024\n\t" \
  "v_smfmac_f32_16x16x64_bf16 v[100:103], v[68:71], a[32:39], v1\n\t" \
  "v_max3_u32 v30, v30, v68, v70\n\t" \
  "s_waitcnt vmcnt(0)\n\t" \
  "ds_write_b32 v11, v24\n\t" \
  "v_smfmac_f32_16x16x64_bf16 v[104:107], v[68:71], a[40:47], v1\n\t" \
  "v_max_u32 v30, v30, v24\n\t" \
  "v_smfmac_f32_16x16x64_bf16 v[108:111], v[68:71], a[48:55], v1\n\t" \
  "v_cmp_eq_u32 vcc, -1, v30\n\t" \
  "v_smfmac_f32_16x16x64_bf16 v[112:115], v[68:71], a[56:63], v1\n\t" \
  "s_cbranch_vccnz A_RETRY_%=\n\t" \
  "A_OK_%=:\n\t" \
  "v_smfmac_f32_16x16x64_bf16 v[116:119], v[64:67], v[164:171], v1\n\t" \
  "s_waitcnt lgkmcnt(0)\n\t" \
  "s_barrier\n\t" \
  "ds_read_b128 v[72:75], v8 offset:0\n\t" \
  "v_smfmac_f32_16x16x64_bf16 v[120:123], v[64:67], v[172:179], v1\n\t" \
  "ds_read_b128 v[76:79], v8 offset:1024\n\t" \
  "v_smfmac_f32_16x16x64_bf16 v[116:119], v[68:71], v[180:187], v1\n\t" \
  "ds_read_b128 v[80:83], v9 offset:0\n\t" \
  "v_smfmac_f32_16x16x64_bf16 v[120:123], v[68:71], v[188:195], v1\n\t" \
  "s_waitcnt lgkmcnt(2)\n\t" \
  "v_smfmac_f32_16x16x64_bf16 v[100:103], v[72:75], a[64:71], v1\n\t" \
  "ds_read_b32 v254, v11 offset:6208\n\t" \
  "v_smfmac_f32_16x16x64_bf16 v[104:107], v[72:75], a[72:79], v1\n\t" \
  "ds_read_b128 v[84:87], v9 offset:1024\n\t" \
  "v_smfmac_f32_16x16x64_bf16 v[108:111], v[72:75], a[80:87], v1\n\t" \
  "ds_read_b128 v[88:91], v10 offset:0\n\t" \
  "v_smfmac_f32_16x16x64_bf16 v[112:115], v[72:75], a[88:95], v1\n\t" \
  "s_waitcnt lgkmcnt(4)\n\t" \
  "v_smfmac_f32_16x16x64_bf16 v[100:103], v[76:79], a[96:103], v1\n\t" \
  "ds_read_b128 v[92:95], v10 offset:1024\n\t" \
  "v_smfmac_f32_16x16x64_bf16 v[104:107], v[76:79], a[104:111], v1\n\t" \
  "ds_read_b128 v[96:99], v6 offset:0\n\t" \
  "v_smfmac_f32_16x16x64_bf16 v[108:111], v[76:79], a[112:119], v1\n\t" \
  "s_add_u32 s62, s58, s59\n\t" \
  "v_smfmac_f32_16x16x64_bf16 v[112:115], v[76:79], a[120:127], v1\n\t" \
  "s_waitcnt lgkmcnt(4)\n\t" \
  "v_smfmac_f32_16x16x64_bf16 v[100:103], v[80:83], a[128:135], v1\n\t" \
  "v_subrev_u32 v254, s58, v254\n\t" \
  "v_smfmac_f32_16x16x64_bf16 v[104:107], v[80:83], a[136:143], v1\n\t" \
  "v_max_i32 v254, 0, v254\n\t" \
  "v_smfmac_f32_16x16x64_bf16 v[108:111], v[80:83], a[144:151], v1\n\t" \
  "v_add_u32 v30, s62, v254\n\t" \
  "v_smfmac_f32_16x16x64_bf16 v[112:115], v[80:83], a[152:159], v1\n\t" \
  "s_waitcnt lgkmcnt(3)\n\t" \
  "v_smfmac_f32_16x16x64_bf16 v[100:103], v[84:87], a[160:167], v1\n\t" \
  "buffer_store_dword v30, v15, s[24:27], s72 offen sc1\n\t" \
  "v_smfmac_f32_16x16x64_bf16 v[104:107], v[84:87], a[168:175], v1\n\t" \
  "ds_read_b128 v[164:167], v5 offset:8192\n\t" \
  "v_smfmac_f32_16x16x64_bf16 v[108:111], v[84:87], a[176:183], v1\n\t" \
  "ds_read_b128 v[168:171], v5 offset:9216\n\t" \
  "v_smfmac_f32_16x16x64_bf16 v[112:115], v[84:87], a[184:191], v1\n\t" \
  "s_waitcnt lgkmcnt(4)\n\t" \
  "v_smfmac_f32_16x16x64_bf16 v[100:103], v[88:91], a[192:199], v1\n\t" \
  "ds_read_b128 v[172:175], v5 offset:10240\n\t" \
  "v_smfmac_f32_16x16x64_bf16 v[104:107], v[88:91], a[200:207], v1\n\t" \
  "ds_read_b128 v[176:179], v5 offset:11264\n\t" \
  "v_smfmac_f32_16x16x64_bf16 v[108:111], v[88:91], a[208:215], v1\n\t" \
  "ds_read_b128 v[180:183], v5 offset:12288\n\t" \
  "v_smfmac_f32_16x16x64_bf16 v[112:115], v[88:91], a[216:223], v1\n\t" \
  "s_waitcnt lgkmcnt(6)\n\t" \
  "v_smfmac_f32_16x16x64_bf16 v[100:103], v[92:95], a[224:231], v1\n\t" \
  "ds_read_b128 v[184:187], v5 offset:13312\n\t" \
  "v_smfmac_f32_16x16x64_bf16 v[104:107], v[92:95], a[232:239], v1\n\t" \
  "ds_read_b128 v[188:191], v5 offset:14336\n\t" \
  "v_smfmac_f32_16x16x64_bf16 v[108:111], v[92:95], a[240:247], v1\n\t" \
  "ds_read_b128 v[192:195], v5 offset:15360\n\t" \
  "v_smfmac_f32_16x16x64_bf16 v[112:115], v[92:95], a[248:255], v1\n\t" \
  "s_waitcnt lgkmcnt(8)\n\t" \
  "v_smfmac_f32_16x16x64_bf16 v[100:103], v[96:99], v[32:39], v1\n\t" \
  "ds_read_b128 v[64:67], v5 offset:16384\n\t" \
  "v_smfmac_f32_16x16x64_bf16 v[104:107], v[96:99], v[40:47], v1\n\t" \
  "ds_read_b128 v[68:71], v5 offset:17408\n\t" \
  "v_smfmac_f32_16x16x64_bf16 v[108:111], v[96:99], v[48:55], v1\n\t" \
  "v_smfmac_f32_16x16x64_bf16 v[112:115], v[96:99], v[56:63], v1\n\t" \
  "s_waitcnt lgkmcnt(4)\n\t" \
  "v_smfmac_f32_16x16x64_bf16 v[116:119], v[72:75], v[164:171], v1\n\t" \
  "v_pk_add_f32 v[100:101], v[100:101], v[102:103]\n\t" \
  "v_pk_add_f32 v[104:105], v[104:105], v[106:107]\n\t" \
  "v_pk_add_f32 v[108:109], v[108:109], v[110:111]\n\t" \
  "v_pk_add_f32 v[112:113], v[112:113], v[114:115]\n\t" \
  "v_cvt_pk_bf16_f32 v24, v100, v104\n\t" \
  "v_cvt_pk_bf16_f32 v25, v108, v112\n\t" \
  "v_cvt_pk_bf16_f32 v26, v101, v105\n\t" \
  "v_cvt_pk_bf16_f32 v27, v109, v113\n\t" \
  "ds_write2_b64 v2, v[24:25], v[26:27] offset1:40\n\t" \
  "v_smfmac_f32_16x16x64_bf16 v[120:123], v[72:75], v[172:179], v1\n\t" \
  "v_smfmac_f32_16x16x64_bf16 v[116:119], v[76:79], v[180:187], v1\n\t" \
  "s_waitcnt lgkmcnt(1)\n\t" \
  "v_smfmac_f32_16x16x64_bf16 v[120:123], v[76:79], v[188:195], v1\n\t" \
  "v_smfmac_f32_16x16x64_bf16 v[116:119], v[80:83], v[64:71], v1\n\t" \
  "s_waitcnt lgkmcnt(0)\n\t" \
  "v_smfmac_f32_16x16x64_bf16 v[120:123], v[80:83], v[196:203], v1\n\t" \
  "s_barrier\n\t" \
  "ds_read_b128 v[164:167], v3 offset:0\n\t" \
  "v_smfmac_f32_16x16x64_bf16 v[116:119], v[84:87], v[204:211], v1\n\t" \
  "ds_read_b128 v[168:171], v3 offset:16\n\t" \
  "v_smfmac_f32_16x16x64_bf16 v[120:123], v[84:87], v[212:219], v1\n\t" \
  "ds_read_b128 v[172:175], v3 offset:128\n\t" \
  "v_smfmac_f32_16x16x64_bf16 v[116:119], v[88:91], v[220:227], v1\n\t" \
  "ds_read_b128 v[176:179], v3 offset:144\n\t" \
  "s_waitcnt lgkmcnt(3)\n\t" \
  "v_mfma_f32_16x16x32_bf16 v[100:103], v[132:135], v[164:167], 0\n\t" \
  "ds_read_b128 v[72:75], v4 offset:0\n\t" \
  "s_cmp_eq_u32 s56, 63\n\t" \
  "v_mfma_f32_16x16x32_bf16 v[104:107], v[148:151], v[164:167], 0\n\t" \
  "ds_read_b128 v[76:79], v4 offset:16\n\t" \
  "s_waitcnt lgkmcnt(4)\n\t" \
  "v_mfma_f32_16x16x32_bf16 v[100:103], v[136:139], v[168:171], v[100:103]\n\t" \
  "ds_read_b128 v[80:83], v4 offset:128\n\t" \
  "s_cbranch_scc1 A_END_%=\n\t" \
  "v_mfma_f32_16x16x32_bf16 v[104:107], v[152:155], v[168:171], v[104:107]\n\t" \
  "ds_read_b128 v[84:87], v4 offset:144\n\t" \
  "s_waitcnt lgkmcnt(4)\n\t" \
  "v_mfma_f32_16x16x32_bf16 v[100:103], v[140:143], v[172:175], v[100:103]\n\t" \
  "ds_read_b128 v[180:183], v5 offset:18432\n\t" \
  "v_mfma_f32_16x16x32_bf16 v[104:107], v[156:159], v[172:175], v[104:107]\n\t" \
  "ds_read_b128 v[184:187], v5 offset:19456\n\t" \
  "v_mfma_f32_16x16x32_bf16 v[100:103], v[144:147], v[176:179], v[100:103]\n\t" \
  "ds_read_b128 v[188:191], v5 offset:20480\n\t" \
  "v_mfma_f32_16x16x32_bf16 v[104:107], v[160:163], v[176:179], v[104:107]\n\t" \
  "ds_read_b128 v[192:195], v5 offset:21504\n\t" \
  "s_waitcnt lgkmcnt(4)\n\t" \
  "v_mfma_f32_16x16x32_bf16 v[108:111], v[72:75], v[164:167], 0\n\t" \
  "s_nop 1\n\t" \
  "v_mfma_f32_16x16x32_bf16 v[108:111], v[76:79], v[168:171], v[108:111]\n\t" \
  "s_nop 0\n\t" \
  "v_max3_f32 v148, v100, v101, v102\n\t" \
  "v_mfma_f32_16x16x32_bf16 v[108:111], v[80:83], v[172:175], v[108:111]\n\t" \
  "v_max3_f32 v149, v104, v105, v106\n\t" \
  "v_max3_f32 v148, v148, v103, v107\n\t" \
  "v_mfma_f32_16x16x32_bf16 v[108:111], v[84:87], v[176:179], v[108:111]\n\t" \
  "v_max_f32 v148, v148, v149\n\t" \
  "v_smfmac_f32_16x16x64_bf16 v[120:123], v[88:91], v[228:235], v1\n\t" \
  "v_smfmac_f32_16x16x64_bf16 v[116:119], v[92:95], v[236:243], v1\n\t" \
  "v_max3_f32 v149, v148, v108, v109\n\t" \
  "v_max3_f32 v148, v148, v108, v109\n\t" \
  "v_smfmac_f32_16x16x64_bf16 v[120:123], v[92:95], v[244:251], v1\n\t" \
  "s_waitcnt lgkmcnt(0)\n\t" \
  "v_permlane16_swap_b32 v148, v149\n\t" \
  "v_max_f32 v150, v148, v149\n\t" \
  "v_max_f32 v151, v148, v149\n\t" \
  "v_smfmac_f32_16x16x64_bf16 v[116:119], v[96:99], v[180:187], v1\n\t" \
  "s_nop 0\n\t" \
  "v_permlane32_swap_b32 v150, v151\n\t" \
  "v_max_f32 v150, v150, v151\n\t" \
  "v_mul_f32 v110, 0xbe0293ee, v150\n\t" \
  "v_pk_fma_f32 v[100:101], v[100:101], s[76:77], v[110:111] op_sel_hi:[1,1,0]\n\t" \
  "v_pk_fma_f32 v[102:103], v[102:103], s[76:77], v[110:111] op_sel_hi:[1,1,0]\n\t" \
  "v_pk_fma_f32 v[104:105], v[104:105], s[76:77], v[110:111] op_sel_hi:[1,1,0]\n\t" \
  "v_pk_fma_f32 v[106:107], v[106:107], s[76:77], v[110:111] op_sel_hi:[1,1,0]\n\t" \
  "v_smfmac_f32_16x16x64_bf16 v[120:123], v[96:99], v[188:195], v1\n\t" \
  "v_exp_f32 v148, v100\n\t" \
  "v_exp_f32 v149, v101\n\t" \
  "v_exp_f32 v150, v102\n\t" \
  "v_exp_f32 v151, v103\n\t" \
  "v_exp_f32 v152, v104\n\t" \
  "v_exp_f32 v153, v105\n\t" \
  "v_exp_f32 v154, v106\n\t" \
  "v_exp_f32 v155, v107\n\t" \
  "v_cvt_pk_bf16_f32 v156, v148, v149\n\t" \
  "v_cvt_pk_bf16_f32 v157, v150, v151\n\t" \
  "v_cvt_pk_bf16_f32 v158, v152, v153\n\t" \
  "v_cvt_pk_bf16_f32 v159, v154, v155\n\t" \
  "v_pk_fma_f32 v[108:109], v[108:109], s[76:77], v[110:111] op_sel_hi:[1,1,0]\n\t" \
  "v_pk_add_f32 v[116:117], v[116:117], v[118:119]\n\t" \
  "v_mfma_f32_16x16x32_bf16 v[112:115], v[16:19], v[156:159], 0\n\t" \
  "v_exp_f32 v148, v108\n\t" \
  "v_mfma_f32_16x16x32_bf16 v[132:135], v[124:127], v[156:159], 0\n\t" \
  "v_exp_f32 v149, v109\n\t" \
  "v_mfma_f32_16x16x32_bf16 v[136:139], v[128:131], v[156:159], 0\n\t" \
  "v_pk_add_f32 v[120:121], v[120:121], v[122:123]\n\t" \
  "v_cvt_pk_bf16_f32 v20, v148, v149\n\t" \
  "v_cvt_pk_bf16_f32 v28, v116, v117\n\t" \
  "v_cvt_pk_bf16_f32 v29, v120, v121\n\t" \
  "v_mfma_f32_16x16x16_bf16 v[112:115], v[16:17], v[20:21], v[112:115]\n\t" \
  "v_cvt_pk_bf16_f32 v23, v148, v149\n\t" \
  "v_mfma_f32_16x16x16_bf16 v[132:135], v[28:29], v[20:21], v[132:135]\n\t" \
  "s_nop 0\n\t" \
  "v_mfma_f32_16x16x16_bf16 v[136:139], v[28:29], v[22:23], v[136:139]\n\t" \
  "s_nop 0\n\t" \
  "v_rcp_f32 v148, v112\n\t" \
  "s_nop 0\n\t" \
  "v_pk_mul_f32 v[132:133], v[132:133], v[148:149] op_sel_hi:[1,0]\n\t" \
  "v_pk_mul_f32 v[134:135], v[134:135], v[148:149] op_sel_hi:[1,0]\n\t" \
  "v_cvt_pk_bf16_f32 v156, v132, v133\n\t" \
  "v_pk_mul_f32 v[136:137], v[136:137], v[148:149] op_sel_hi:[1,0]\n\t" \
  "v_pk_mul_f32 v[138:139], v[138:139], v[148:149] op_sel_hi:[1,0]\n\t" \
  "v_cvt_pk_bf16_f32 v157, v134, v135\n\t" \
  "v_cvt_pk_bf16_f32 v158, v136, v137\n\t" \
  "v_cvt_pk_bf16_f32 v159, v138, v139\n\t" \
  "buffer_store_dwordx4 v[156:159], v13, s[24:27], s70 offen sc1\n\t" \
  "s_memtime s[84:85]\n\t" \
  "s_memrealtime s[86:87]\n\t" \
  "v_readfirstlane_b32 s62, v254\n\t" \
  "s_add_u32 s58, s58, s62\n\t" \
  "s_add_u32 s56, s56, 1\n\t" \
  "s_mov_b32 s63, 0\n\t" \
  "s_add_u32 s74, s72, s52\n\t" \
  "s_barrier\n\t" \
  "ds_read_b128 v[68:71], v14 offset:0\n\t" \
  "v_lshlrev_b32 v254, 1, v12\n\t" \
  "buffer_store_dwordx2 v[28:29], v254, s[24:27], s79 offen sc1\n\t" \
  "s_waitcnt lgkmcnt(0)\n\t" \
  "buffer_store_dwordx4 v[68:71], v0, s[24:27], s78 offen sc1\n\t" \
  "buffer_load_dword v31, v0, s[24:27], s74 offen sc1\n\t" \
  "v_min_u32 v30, 560, v0\n\t" \
  "buffer_load_dword v24, v30, s[24:27], s53 offen sc1\n\t" \
  "A_KVC_%=:\n\t" \
  "s_add_u32 s60, s56, 1\n\t" \
  "s_min_u32 s60, s60, 62\n\t" \
  "v_readfirstlane_b32 s61, v252\n\t" \
  "v_readfirstlane_b32 s62, v253\n\t" \
  "s_cmp_eq_u32 s62, s55\n\t" \
  "s_cselect_b32 s61, s61, -1\n\t" \
  "s_cmp_gt_i32 s61, s60\n\t" \
  "s_cbranch_scc1 A_KVOK_%=\n\t" \
  "s_cmp_lg_u32 s64, 0\n\t" \
  "s_cbranch_scc1 A_KVOK_%=\n\t" \
  "s_add_u32 s63, s63, 1\n\t" \
  "s_cmp_gt_u32 s63, 20000\n\t" \
  "s_cselect_b32 s64, 1, 0\n\t" \
  "buffer_load_dwordx2 v[252:253], off, s[24:27], s47 sc1\n\t" \
  "s_waitcnt vmcnt(0)\n\t" \
  "s_branch A_KVC_%=\n\t" \
  "A_KVOK_%=:\n\t" \
  "s_add_u32 s60, s56, 1\n\t" \
  "s_min_u32 s61, s60, 62\n\t" \
  "s_lshl_b32 s61, s61, 19\n\t" \
  "s_add_u32 s73, s43, s61\n\t" \
  "s_and_b32 s60, s60, 1\n\t" \
  "s_lshl_b32 s60, s60, 14\n\t" \
  "s_add_u32 m0, s60, s54\n\t" \
  "s_nop 0\n\t" \
  "buffer_load_dwordx4 v0, s[24:27], s73 offen offset:0 sc1 lds\n\t" \
  "buffer_load_dwordx4 v0, s[24:27], s73 offen offset:1024 sc1 lds\n\t" \
  "buffer_load_dwordx4 v0, s[24:27], s73 offen offset:2048 sc1 lds\n\t" \
  "buffer_load_dwordx4 v0, s[24:27], s73 offen offset:3072 sc1 lds\n\t" \
  "buffer_load_dwordx2 v[252:253], off, s[24:27], s47 sc1\n\t" \
  "v_mov_b32 v64, -1\n\t" \
  "v_mov_b32 v65, -1\n\t" \
  "v_mov_b32 v66, -1\n\t" \
  "v_mov_b32 v67, -1\n\t" \
  "buffer_store_dwordx4 v[64:67], v13, s[24:27], s71 offen sc1\n\t" \
  "s_and_b32 s60, s56, 1\n\t" \
  "s_lshl_b32 s60, s60, 14\n\t" \
  "v_add_u32 v254, s60, v6\n\t" \
  "v_add_u32 v30, s60, v7\n\t" \
  "ds_read_b128 v[132:135], v254 offset:7232\n\t" \
  "ds_read_b128 v[136:139], v254 offset:8256\n\t" \
  "ds_read_b128 v[140:143], v254 offset:9280\n\t" \
  "ds_read_b128 v[144:147], v254 offset:10304\n\t" \
  "ds_read_b128 v[148:151], v254 offset:11328\n\t" \
  "ds_read_b128 v[152:155], v254 offset:12352\n\t" \
  "ds_read_b128 v[156:159], v254 offset:13376\n\t" \
  "ds_read_b128 v[160:163], v254 offset:14400\n\t" \
  "ds_read_b128 v[124:127], v30 offset:23616\n\t" \
  "ds_read_b128 v[128:131], v30 offset:24640\n\t" \
  "s_waitcnt lgkmcnt(0)\n\t" \
  "s_memrealtime s[86:87]\n\t" \
  "s_waitcnt lgkmcnt(0)\n\t" \
  "A_MS_%=:\n\t" \
  "s_memtime s[88:89]\n\t" \
  "s_memrealtime s[66:67]\n\t" \
  "s_waitcnt lgkmcnt(0)\n\t" \
  "s_cmp_eq_u32 s66, s86\n\t" \
  "s_cbranch_scc1 A_MS_%=\n\t" \
  "s_sub_u32 s60, s66, s58\n\t" \
  "s_lshl_b32 s60, s60, 4\n\t" \
  "s_sub_u32 s61, s88, s84\n\t" \
  "s_mul_i32 s61, s61, 11\n\t" \
  "s_lshr_b32 s61, s61, 4\n\t" \
  "s_sub_u32 s60, s60, s61\n\t" \
  "s_add_u32 s60, s60, 20\n\t" \
  "s_sub_u32 s60, s60, s80\n\t" \
  "s_min_i32 s60, s60, 64\n\t" \
  "s_max_i32 s60, s60, -96\n\t" \
  "s_ashr_i32 s60, s60, 2\n\t" \
  "s_add_u32 s80, s80, s60\n\t" \
  "s_and_b32 s60, s56, 3\n\t" \
  "s_mul_i32 s61, s60, 180224\n\t" \
  "s_add_u32 s68, s41, s61\n\t" \
  "s_add_u32 s69, s42, s61\n\t" \
  "s_add_u32 s70, s40, s61\n\t" \
  "s_add_u32 s72, s46, s61\n\t" \
  "s_xor_b32 s60, s60, 2\n\t" \
  "s_mul_i32 s61, s60, 180224\n\t" \
  "s_add_u32 s71, s40, s61\n\t" \
  "s_lshl_b32 s60, s56, 18\n\t" \
  "s_add_u32 s78, s44, s60\n\t" \
  "s_add_u32 s79, s45, s60\n\t" \
  "s_add_u32 s60, s80, 8\n\t" \
  "s_lshr_b32 s59, s60, 4\n\t" \
  "s_waitcnt vmcnt(6)\n\t" \
  "s_barrier\n\t" \
  "s_bitcmp1_b32 s49, 1\n\t" \
  "s_cbranch_scc0 A_Fm_%=\n\t" \
  "v_mov_b32 v26, s56\n\t" \
  "v_mov_b32 v27, s55\n\t" \
  "s_mov_b64 exec, 1\n\t" \
  "buffer_store_dwordx2 v[26:27], off, s[24:27], s50 sc1\n\t" \
  "s_mov_b64 exec, -1\n\t" \
  "A_Fm_%=:\n\t" \
  "s_mov_b32 s63, 0\n\t" \
  "A_AN_%=:\n\t" \
  "v_subrev_u32 v31, s81, v31\n\t" \
  "ds_read_b128 v[164:167], v5 offset:0\n\t" \
  "v_max_i32_dpp v24, v24, v24 row_ror:1 row_mask:0xf bank_mask:0xf\n\t" \
  "v_mov_b64 v[100:101], 0\n\t" \
  "v_mov_b64 v[102:103], 0\n\t" \
  "ds_read_b128 v[168:171], v5 offset:1024\n\t" \
  "v_mov_b64 v[116:117], 0\n\t" \
  "v_max_u32_dpp v31, v31, v31 row_ror:1 row_mask:0xf bank_mask:0xf\n\t" \
  "ds_read_b128 v[172:175], v5 offset:2048\n\t" \
  "v_max_i32_dpp v24, v24, v24 row_ror:2 row_mask:0xf bank_mask:0xf\n\t" \
  "v_mov_b64 v[104:105], 0\n\t" \
  "v_mov_b64 v[106:107], 0\n\t" \
  "ds_read_b128 v[176:179], v5 offset:3072\n\t" \
  "v_mov_b64 v[118:119], 0\n\t" \
  "v_max_u32_dpp v31, v31, v31 row_ror:2 row_mask:0xf bank_mask:0xf\n\t" \
  "ds_read_b128 v[180:183], v5 offset:4096\n\t" \
  "v_max_i32_dpp v24, v24, v24 row_ror:4 row_mask:0xf bank_mask:0xf\n\t" \
  "v_mov_b64 v[108:109], 0\n\t" \
  "v_mov_b64 v[110:111], 0\n\t" \
  "ds_read_b128 v[184:187], v5 offset:5120\n\t" \
  "v_mov_b64 v[120:121], 0\n\t" \
  "v_max_u32_dpp v31, v31, v31 row_ror:4 row_mask:0xf bank_mask:0xf\n\t" \
  "ds_read_b128 v[188:191], v5 offset:6144\n\t" \
  "v_max_i32_dpp v24, v24, v24 row_ror:8 row_mask:0xf bank_mask:0xf\n\t" \
  "v_mov_b64 v[112:113], 0\n\t" \
  "v_mov_b64 v[114:115], 0\n\t" \
  "ds_read_b128 v[192:195], v5 offset:7168\n\t" \
  "v_mov_b64 v[122:123], 0\n\t" \
  "v_max_u32_dpp v31, v31, v31 row_ror:8 row_mask:0xf bank_mask:0xf\n\t" \
  "s_nop 0\n\t" \
  "v_readlane_b32 s60, v31, 0\n\t" \
  "v_readlane_b32 s61, v31, 16\n\t" \
  "v_readlane_b32 s66, v31, 32\n\t" \
  "v_readlane_b32 s67, v31, 48\n\t" \
  "s_max_u32 s60, s60, s61\n\t" \
  "s_max_u32 s66, s66, s67\n\t" \
  "s_max_u32 s60, s60, s66\n\t" \
  "s_cmp_le_u32 s60, 0xffff\n\t" \
  "s_cbranch_scc1 A_ANOK_%=\n\t" \
  "s_mov_b32 s60, 0\n\t" \
  "s_cmp_lg_u32 s64, 0\n\t" \
  "s_cbranch_scc1 A_ANOK_%=\n\t" \
  "s_add_u32 s63, s63, 1\n\t" \
  "s_cmp_gt_u32 s63, 20000\n\t" \
  "s_cselect_b32 s64, 1, 0\n\t" \
  "s_sleep 1\n\t" \
  "buffer_load_dword v31, v0, s[24:27], s74 offen sc1\n\t" \
  "s_waitcnt vmcnt(0)\n\t" \
  "s_branch A_AN_%=\n\t" \
  "A_ANOK_%=:\n\t" \
  "s_add_u32 s83, s81, s60\n\t" \
  "v_readlane_b32 s60, v24, 0\n\t" \
  "v_readlane_b32 s61, v24, 16\n\t" \
  "v_readlane_b32 s62, v24, 32\n\t" \
  "s_max_i32 s60, s60, s61\n\t" \
  "s_max_i32 s62, s60, s62\n\t" \
  "s_min_i32 s62, s62, 0x200\n\t" \
  "s_add_u32 s62, s62, s82\n\t" \
  "s_add_u32 s57, s83, s62\n\t" \
  "s_add_u32 s81, s83, 16\n\t" \
  "s_mov_b32 s58, s57\n\t" \
  "s_branch A_LOOP_%=\n\t" \
  "A_RETRY_%=:\n\t" \
  "s_cmp_lg_u32 s64, 0\n\t" \
  "s_cbranch_scc1 A_OK_%=\n\t" \
  "s_add_u32 s82, s82, 0\n\t" \
  "s_waitcnt lgkmcnt(0)\n\t" \
  "buffer_load_dwordx4 v[64:67], v0, s[24:27], s68 offen sc1\n\t" \
  "buffer_load_dwordx4 v[68:71], v0, s[24:27], s68 offen offset:1024 sc1\n\t" \
  "buffer_load_dword v24, v12, s[24:27], s69 offen sc1\n\t" \
  "s_nop 1\n\t" \
  "v_mov_b64 v[100:101], 0\n\t" \
  "v_mov_b64 v[102:103], 0\n\t" \
  "v_mov_b64 v[104:105], 0\n\t" \
  "v_mov_b64 v[106:107], 0\n\t" \
  "v_mov_b64 v[108:109], 0\n\t" \
  "v_mov_b64 v[110:111], 0\n\t" \
  "v_mov_b64 v[112:113], 0\n\t" \
  "v_mov_b64 v[114:115], 0\n\t" \
  "s_waitcnt vmcnt(0)\n\t" \
  "s_branch A_SCHK_%=\n\t" \
  "A_SLOW_%=:\n\t" \
  "buffer_load_dwordx4 v[64:67], v0, s[24:27], s68 offen sc1\n\t" \
  "buffer_load_dwordx4 v[68:71], v0, s[24:27], s68 offen offset:1024 sc1\n\t" \
  "buffer_load_dword v24, v12, s[24:27], s69 offen sc1\n\t" \
  "s_waitcnt vmcnt(0)\n\t" \
  "A_SCHK_%=:\n\t" \
  "s_memrealtime s[66:67]\n\t" \
  "v_max3_u32 v30, v64, v66, v68\n\t" \
  "v_max3_u32 v30, v30, v70, v24\n\t" \
  "v_cmp_eq_u32 vcc, -1, v30\n\t" \
  "s_add_u32 s63, s63, 1\n\t" \
  "s_cmp_gt_u32 s63, 20000\n\t" \
  "s_cselect_b32 s64, 1, s64\n\t" \
  "s_cbranch_vccz A_SGO_%=\n\t" \
  "s_cmp_eq_u32 s64, 0\n\t" \
  "s_cbranch_scc1 A_SLOW_%=\n\t" \
  "A_SGO_%=:\n\t" \
  "ds_write_b128 v7, v[64:67]\n\t" \
  "ds_write_b128 v7, v[68:71] offset:1024\n\t" \
  "ds_write_b32 v11, v24\n\t" \
  "v_smfmac_f32_16x16x64_bf16 v[100:103], v[64:67], a[0:7], v1\n\t" \
  "v_smfmac_f32_16x16x64_bf16 v[104:107], v[64:67], a[8:15], v1\n\t" \
  "v_smfmac_f32_16x16x64_bf16 v[108:111], v[64:67], a[16:23], v1\n\t" \
  "v_smfmac_f32_16x16x64_bf16 v[112:115], v[64:67], a[24:31], v1\n\t" \
  "s_waitcnt lgkmcnt(0)\n\t" \
  "s_sub_u32 s58, s66, 18\n\t" \
  "v_mov_b32 v254, s58\n\t" \
  "s_mov_b32 s60, 104512\n\t" \
  "v_add_u32 v30, s60, v12\n\t" \
  "ds_write_b32 v30, v254\n\t" \
  "v_smfmac_f32_16x16x64_bf16 v[100:103], v[68:71], a[32:39], v1\n\t" \
  "v_smfmac_f32_16x16x64_bf16 v[104:107], v[68:71], a[40:47], v1\n\t" \
  "v_smfmac_f32_16x16x64_bf16 v[108:111], v[68:71], a[48:55], v1\n\t" \
  "v_smfmac_f32_16x16x64_bf16 v[112:115], v[68:71], a[56:63], v1\n\t" \
  "s_branch A_OK_%=\n\t" \
  "A_END_%=:\n\t" \
  "ds_read_b128 v[180:183], v5 offset:18432\n\t" \
  "ds_read_b128 v[184:187], v5 offset:19456\n\t" \
  "ds_read_b128 v[188:191], v5 offset:20480\n\t" \
  "ds_read_b128 v[192:195], v5 offset:21504\n\t" \
  "v_smfmac_f32_16x16x64_bf16 v[120:123], v[88:91], v[228:235], v1\n\t" \
  "v_smfmac_f32_16x16x64_bf16 v[116:119], v[92:95], v[236:243], v1\n\t" \
  "v_smfmac_f32_16x16x64_bf16 v[120:123], v[92:95], v[244:251], v1\n\t" \
  "s_waitcnt lgkmcnt(0)\n\t" \
  "v_smfmac_f32_16x16x64_bf16 v[116:119], v[96:99], v[180:187], v1\n\t" \
  "v_smfmac_f32_16x16x64_bf16 v[120:123], v[96:99], v[188:195], v1\n\t" \
  "s_nop 7\n\t" \
  "v_pk_add_f32 v[116:117], v[116:117], v[118:119]\n\t" \
  "v_pk_add_f32 v[120:121], v[120:121], v[122:123]\n\t" \
  "v_cvt_pk_bf16_f32 v28, v116, v117\n\t" \
  "v_cvt_pk_bf16_f32 v29, v120, v121\n\t" \
  "ds_read_b128 v[68:71], v14 offset:0\n\t" \
  "v_lshlrev_b32 v254, 1, v12\n\t" \
  "buffer_store_dwordx2 v[28:29], v254, s[24:27], s79 offen sc1\n\t" \
  "s_waitcnt lgkmcnt(0)\n\t" \
  "buffer_store_dwordx4 v[68:71], v0, s[24:27], s78 offen sc1\n\t" \
  "s_add_u32 s56, s56, 1\n\t" \
  "s_waitcnt vmcnt(0)\n\t" \
  "s_barrier\n\t" \
  "s_bitcmp1_b32 s49, 1\n\t" \
  "s_cbranch_scc0 A_Fe_%=\n\t" \
  "v_mov_b32 v26, s56\n\t" \
  "v_mov_b32 v27, s55\n\t" \
  "s_mov_b64 exec, 1\n\t" \
  "buffer_store_dwordx2 v[26:27], off, s[24:27], s50 sc1\n\t" \
  "s_mov_b64 exec, -1\n\t" \
  "A_Fe_%=:\n\t" \
  "v_mov_b32 v100, s90\n\t" \
  "v_mov_b32 v101, s80\n\t" \
  "v_mov_b32 v102, s82\n\t" \
  "v_mov_b32 v103, s64\n\t" \
  "s_nop 7\n\t" \
  ""
#define A_ASM_CLOB "v16", "v17", "v18", "v19", "v20", "v21", "v22", "v23", "v24", "v25", "v26", "v27", "v28", "v29", "v30", "v31", "v64", "v65", "v66", "v67", "v68", "v69", "v70", "v71", "v72", "v73", "v74", "v75", "v76", "v77", "v78", "v79", "v80", "v81", "v82", "v83", "v84", "v85", "v86", "v87", "v88", "v89", "v90", "v91", "v92", "v93", "v94", "v95", "v96", "v97", "v98", "v99", "v104", "v105", "v106", "v107", "v108", "v109", "v110", "v111", "v112", "v113", "v114", "v115", "v116", "v117", "v118", "v119", "v120", "v121", "v122", "v123", "v124", "v125", "v126", "v127", "v128", "v129", "v130", "v131", "v132", "v133", "v134", "v135", "v136", "v137", "v138", "v139", "v140", "v141", "v142", "v143", "v144", "v145", "v146", "v147", "v148", "v149", "v150", "v151", "v152", "v153", "v154", "v155", "v156", "v157", "v158", "v159", "v160", "v161", "v162", "v163", "v164", "v165", "v166", "v167", "v168", "v169", "v170", "v171", "v172", "v173", "v174", "v175", "v176", "v177", "v178", "v179", "v180", "v181", "v182", "v183", "v184", "v185", "v186", "v187", "v188", "v189", "v190", "v191", "v192", "v193", "v194", "v195", "v252", "v253", "v254", "s56", "s57", "s58", "s59", "s60", "s61", "s62", "s63", "s64", "s65", "s66", "s67", "s68", "s69", "s70", "s71", "s72", "s73", "s74", "s75", "s76", "s77", "s78", "s79", "s80", "s81", "s82", "s83", "s84", "s85", "s86", "s87", "s88", "s89", "s90", "s91", "s92", "s93", "s94", "s95", "s96", "s97", "s98", "s99", "vcc", "scc", "memory"
#define O_ASM_TEXT \
  "s_mov_b32 s56, 0\n\t" \
  "s_mov_b32 s57, s43\n\t" \
  "s_mov_b32 s81, s43\n\t" \
  "s_mov_b32 s64, 0\n\t" \
  "s_mov_b32 s68, 6\n\t" \
  "s_mov_b32 s82, 164\n\t" \
  "s_mov_b32 s80, 1184\n\t" \
  "s_mov_b32 s59, 74\n\t" \
  "s_mov_b32 s90, 0\n\t" \
  "s_mov_b32 s91, 0\n\t" \
  "s_mov_b32 s92, 0\n\t" \
  "s_mov_b32 s93, 0\n\t" \
  "s_mov_b32 s94, 0\n\t" \
  "s_mov_b32 s95, 0\n\t" \
  "s_mov_b32 s96, 0\n\t" \
  "s_mov_b32 s97, 0\n\t" \
  "s_mov_b32 s98, 0\n\t" \
  "s_mov_b32 s99, 0\n\t" \
  "v_mov_b32 v74, -1\n\t" \
  "v_mov_b32 v75, -1\n\t" \
  "O_LOOP_%=:\n\t" \
  "s_and_b32 s60, s56, 3\n\t" \
  "s_mul_i32 s61, s60, 180224\n\t" \
  "s_add_u32 s69, s40, s61\n\t" \
  "s_add_u32 s70, s69, 4096\n\t" \
  "s_add_u32 s73, s42, s61\n\t" \
  "s_add_u32 s60, s60, 1\n\t" \
  "s_and_b32 s60, s60, 3\n\t" \
  "s_mul_i32 s61, s60, 180224\n\t" \
  "s_add_u32 s71, s41, s61\n\t" \
  "s_xor_b32 s60, s60, 2\n\t" \
  "s_mul_i32 s61, s60, 180224\n\t" \
  "s_add_u32 s72, s41, s61\n\t" \
  "OA_TK_%=:\n\t" \
  "s_memrealtime s[66:67]\n\t" \
  "s_waitcnt lgkmcnt(0)\n\t" \
  "s_sub_u32 s91, s57, s66\n\t" \
  "s_cmp_gt_i32 s91, 3\n\t" \
  "s_cbranch_scc1 OA_TK_%=\n\t" \
  "s_cmp_lt_i32 s91, 1\n\t" \
  "s_cbranch_scc1 OA_TK0_%=\n\t" \
  "s_nop 3\n\t" \
  "s_cmp_lt_i32 s91, 2\n\t" \
  "s_cbranch_scc1 OA_TKG_%=\n\t" \
  "s_nop 3\n\t" \
  "s_cmp_lt_i32 s91, 3\n\t" \
  "s_cbranch_scc1 OA_TKG_%=\n\t" \
  "s_nop 4\n\t" \
  "s_branch OA_TKG_%=\n\t" \
  "OA_TK0_%=:\n\t" \
  "OA_TKG_%=:\n\t" \
  "buffer_load_dword v73, v0, s[24:27], s73 offen sc1\n\t" \
  "s_waitcnt vmcnt(0)\n\t" \
  "s_mov_b32 s63, 0\n\t" \
  "O_AN_%=:\n\t" \
  "v_subrev_u32 v73, s81, v73\n\t" \
  "s_nop 1\n\t" \
  "v_max_u32_dpp v73, v73, v73 row_ror:1 row_mask:0xf bank_mask:0xf\n\t" \
  "s_nop 1\n\t" \
  "v_max_u32_dpp v73, v73, v73 row_ror:2 row_mask:0xf bank_mask:0xf\n\t" \
  "s_nop 1\n\t" \
  "v_max_u32_dpp v73, v73, v73 row_ror:4 row_mask:0xf bank_mask:0xf\n\t" \
  "s_nop 1\n\t" \
  "v_max_u32_dpp v73, v73, v73 row_ror:8 row_mask:0xf bank_mask:0xf\n\t" \
  "s_nop 0\n\t" \
  "v_readlane_b32 s60, v73, 0\n\t" \
  "v_readlane_b32 s61, v73, 16\n\t" \
  "v_readlane_b32 s66, v73, 32\n\t" \
  "v_readlane_b32 s67, v73, 48\n\t" \
  "s_max_u32 s60, s60, s61\n\t" \
  "s_max_u32 s66, s66, s67\n\t" \
  "s_max_u32 s60, s60, s66\n\t" \
  "s_cmp_le_u32 s60, 0xffff\n\t" \
  "s_cbranch_scc1 O_ANOK_%=\n\t" \
  "s_mov_b32 s60, 0\n\t" \
  "s_cmp_lg_u32 s64, 0\n\t" \
  "s_cbranch_scc1 O_ANOK_%=\n\t" \
  "s_add_u32 s63, s63, 1\n\t" \
  "s_cmp_gt_u32 s63, 20000\n\t" \
  "s_cselect_b32 s64, 1, 0\n\t" \
  "s_sleep 1\n\t" \
  "buffer_load_dword v73, v0, s[24:27], s73 offen sc1\n\t" \
  "s_waitcnt vmcnt(0)\n\t" \
  "s_branch O_AN_%=\n\t" \
  "O_ANOK_%=:\n\t" \
  "s_add_u32 s83, s81, s60\n\t" \
  "s_add_u32 s82, s82, 1\n\t" \
  "s_min_u32 s82, s82, s60\n\t" \
  "s_add_u32 s65, s83, s68\n\t" \
  "s_mov_b32 s63, 0\n\t" \
  "s_add_u32 s81, s83, 16\n\t" \
  "v_mov_b64 v[40:41], 0\n\t" \
  "v_mov_b64 v[42:43], 0\n\t" \
  "v_mov_b64 v[44:45], 0\n\t" \
  "v_mov_b64 v[46:47], 0\n\t" \
  "v_mov_b64 v[48:49], 0\n\t" \
  "v_mov_b64 v[50:51], 0\n\t" \
  "v_mov_b64 v[52:53], 0\n\t" \
  "v_mov_b64 v[54:55], 0\n\t" \
  "v_mov_b32 v72, 0\n\t" \
  "OB_TK_%=:\n\t" \
  "s_memrealtime s[66:67]\n\t" \
  "s_waitcnt lgkmcnt(0)\n\t" \
  "s_sub_u32 s91, s65, s66\n\t" \
  "s_cmp_gt_i32 s91, 3\n\t" \
  "s_cbranch_scc1 OB_TK_%=\n\t" \
  "s_cmp_lt_i32 s91, 1\n\t" \
  "s_cbranch_scc1 OB_TK0_%=\n\t" \
  "s_nop 3\n\t" \
  "s_cmp_lt_i32 s91, 2\n\t" \
  "s_cbranch_scc1 OB_TKG_%=\n\t" \
  "s_nop 3\n\t" \
  "s_cmp_lt_i32 s91, 3\n\t" \
  "s_cbranch_scc1 OB_TKG_%=\n\t" \
  "s_nop 4\n\t" \
  "s_branch OB_TKG_%=\n\t" \
  "OB_TK0_%=:\n\t" \
  "OB_TKG_%=:\n\t" \
  "O_L1_%=:\n\t" \
  "buffer_load_dwordx4 v[8:11], v0, s[24:27], s69 offen offset:0 sc1\n\t" \
  "s_nop 11\n\t" \
  "buffer_load_dwordx4 v[12:15], v0, s[24:27], s69 offen offset:1024 sc1\n\t" \
  "s_nop 11\n\t" \
  "buffer_load_dwordx4 v[16:19], v0, s[24:27], s69 offen offset:2048 sc1\n\t" \
  "s_nop 11\n\t" \
  "buffer_load_dwordx4 v[20:23], v0, s[24:27], s69 offen offset:3072 sc1\n\t" \
  "s_nop 11\n\t" \
  "buffer_load_dwordx4 v[24:27], v0, s[24:27], s70 offen offset:0 sc1\n\t" \
  "s_nop 11\n\t" \
  "buffer_load_dwordx4 v[28:31], v0, s[24:27], s70 offen offset:1024 sc1\n\t" \
  "s_nop 11\n\t" \
  "buffer_load_dwordx4 v[32:35], v0, s[24:27], s70 offen offset:2048 sc1\n\t" \
  "s_nop 11\n\t" \
  "buffer_load_dwordx4 v[36:39], v0, s[24:27], s70 offen offset:3072 sc1\n\t" \
  "s_waitcnt vmcnt(7)\n\t" \
  "v_smfmac_f32_16x16x64_bf16 v[40:43], v[8:11], a[0:7], v1\n\t" \
  "v_max3_u32 v72, v72, v8, v10\n\t" \
  "v_smfmac_f32_16x16x64_bf16 v[44:47], v[8:11], a[8:15], v1\n\t" \
  "v_cmp_eq_u32 vcc, -1, v72\n\t" \
  "v_smfmac_f32_16x16x64_bf16 v[48:51], v[8:11], a[16:23], v1\n\t" \
  "s_cbranch_vccnz O_RETRY_%=\n\t" \
  "v_smfmac_f32_16x16x64_bf16 v[52:55], v[8:11], a[24:31], v1\n\t" \
  "s_waitcnt vmcnt(6)\n\t" \
  "v_smfmac_f32_16x16x64_bf16 v[40:43], v[12:15], a[32:39], v1\n\t" \
  "v_max3_u32 v72, v72, v12, v14\n\t" \
  "v_smfmac_f32_16x16x64_bf16 v[44:47], v[12:15], a[40:47], v1\n\t" \
  "v_smfmac_f32_16x16x64_bf16 v[48:51], v[12:15], a[48:55], v1\n\t" \
  "v_smfmac_f32_16x16x64_bf16 v[52:55], v[12:15], a[56:63], v1\n\t" \
  "s_waitcnt vmcnt(5)\n\t" \
  "v_smfmac_f32_16x16x64_bf16 v[40:43], v[16:19], a[64:71], v1\n\t" \
  "v_max3_u32 v72, v72, v16, v18\n\t" \
  "v_smfmac_f32_16x16x64_bf16 v[44:47], v[16:19], a[72:79], v1\n\t" \
  "v_smfmac_f32_16x16x64_bf16 v[48:51], v[16:19], a[80:87], v1\n\t" \
  "v_smfmac_f32_16x16x64_bf16 v[52:55], v[16:19], a[88:95], v1\n\t" \
  "s_waitcnt vmcnt(4)\n\t" \
  "v_smfmac_f32_16x16x64_bf16 v[40:43], v[20:23], a[96:103], v1\n\t" \
  "v_max3_u32 v72, v72, v20, v22\n\t" \
  "v_smfmac_f32_16x16x64_bf16 v[44:47], v[20:23], a[104:111], v1\n\t" \
  "v_cmp_eq_u32 vcc, -1, v72\n\t" \
  "v_smfmac_f32_16x16x64_bf16 v[48:51], v[20:23], a[112:119], v1\n\t" \
  "s_cbranch_vccnz O_RETRY_%=\n\t" \
  "v_smfmac_f32_16x16x64_bf16 v[52:55], v[20:23], a[120:127], v1\n\t" \
  "s_waitcnt vmcnt(3)\n\t" \
  "v_smfmac_f32_16x16x64_bf16 v[40:43], v[24:27], a[128:135], v1\n\t" \
  "v_max3_u32 v72, v72, v24, v26\n\t" \
  "v_smfmac_f32_16x16x64_bf16 v[44:47], v[24:27], a[136:143], v1\n\t" \
  "v_smfmac_f32_16x16x64_bf16 v[48:51], v[24:27], a[144:151], v1\n\t" \
  "v_smfmac_f32_16x16x64_bf16 v[52:55], v[24:27], a[152:159], v1\n\t" \
  "s_waitcnt vmcnt(2)\n\t" \
  "v_smfmac_f32_16x16x64_bf16 v[40:43], v[28:31], a[160:167], v1\n\t" \
  "v_max3_u32 v72, v72, v28, v30\n\t" \
  "v_smfmac_f32_16x16x64_bf16 v[44:47], v[28:31], a[168:175], v1\n\t" \
  "v_smfmac_f32_16x16x64_bf16 v[48:51], v[28:31], a[176:183], v1\n\t" \
  "v_smfmac_f32_16x16x64_bf16 v[52:55], v[28:31], a[184:191], v1\n\t" \
  "s_waitcnt vmcnt(1)\n\t" \
  "v_smfmac_f32_16x16x64_bf16 v[40:43], v[32:35], a[192:199], v1\n\t" \
  "v_max3_u32 v72, v72, v32, v34\n\t" \
  "v_smfmac_f32_16x16x64_bf16 v[44:47], v[32:35], a[200:207], v1\n\t" \
  "v_smfmac_f32_16x16x64_bf16 v[48:51], v[32:35], a[208:215], v1\n\t" \
  "v_smfmac_f32_16x16x64_bf16 v[52:55], v[32:35], a[216:223], v1\n\t" \
  "s_waitcnt vmcnt(0)\n\t" \
  "v_smfmac_f32_16x16x64_bf16 v[40:43], v[36:39], a[224:231], v1\n\t" \
  "v_max3_u32 v72, v72, v36, v38\n\t" \
  "v_smfmac_f32_16x16x64_bf16 v[44:47], v[36:39], a[232:239], v1\n\t" \
  "v_cmp_eq_u32 vcc, -1, v72\n\t" \
  "v_smfmac_f32_16x16x64_bf16 v[48:51], v[36:39], a[240:247], v1\n\t" \
  "v_smfmac_f32_16x16x64_bf16 v[52:55], v[36:39], a[248:255], v1\n\t" \
  "s_cbranch_vccnz O_RETRY_%=\n\t" \
  "O_OK_%=:\n\t" \
  "s_nop 0\n\t" \
  "v_pk_add_f32 v[40:41], v[40:41], v[42:43]\n\t" \
  "v_pk_add_f32 v[44:45], v[44:45], v[46:47]\n\t" \
  "v_pk_add_f32 v[48:49], v[48:49], v[50:51]\n\t" \
  "v_pk_add_f32 v[52:53], v[52:53], v[54:55]\n\t" \
  "v_permlane32_swap_b32 v40, v44\n\t" \
  "v_permlane32_swap_b32 v41, v45\n\t" \
  "v_permlane32_swap_b32 v48, v52\n\t" \
  "v_permlane32_swap_b32 v49, v53\n\t" \
  "v_pk_add_f32 v[40:41], v[40:41], v[52:53]\n\t" \
  "ds_write2_b32 v2, v40, v41 offset1:32\n\t" \
  "s_bitcmp1_b32 s44, 1\n\t" \
  "s_waitcnt lgkmcnt(0)\n\t" \
  "s_barrier\n\t" \
  "s_cbranch_scc0 O_NEXT_%=\n\t" \
  "ds_read_b128 v[56:59], v3 offset:0\n\t" \
  "ds_read_b128 v[60:63], v3 offset:512\n\t" \
  "ds_read_b128 v[64:67], v3 offset:1024\n\t" \
  "ds_read_b128 v[68:71], v3 offset:1536\n\t" \
  "s_waitcnt lgkmcnt(2)\n\t" \
  "v_pk_add_f32 v[56:57], v[56:57], v[60:61]\n\t" \
  "v_pk_add_f32 v[58:59], v[58:59], v[62:63]\n\t" \
  "s_waitcnt lgkmcnt(1)\n\t" \
  "v_pk_add_f32 v[56:57], v[56:57], v[64:65]\n\t" \
  "v_pk_add_f32 v[58:59], v[58:59], v[66:67]\n\t" \
  "s_waitcnt lgkmcnt(0)\n\t" \
  "v_pk_add_f32 v[56:57], v[56:57], v[68:69]\n\t" \
  "v_pk_add_f32 v[58:59], v[58:59], v[70:71]\n\t" \
  "v_cvt_pk_bf16_f32 v56, v56, v57\n\t" \
  "v_cvt_pk_bf16_f32 v57, v58, v59\n\t" \
  "buffer_store_dwordx2 v[56:57], v4, s[24:27], s71 offen sc1\n\t" \
  "s_memtime s[84:85]\n\t" \
  "s_memrealtime s[86:87]\n\t" \
  "buffer_store_dwordx2 v[74:75], v4, s[24:27], s72 offen sc1\n\t" \
  "s_bitcmp1_b32 s44, 2\n\t" \
  "s_cbranch_scc0 O_NEXT_%=\n\t" \
  "s_waitcnt lgkmcnt(0)\n\t" \
  "O_MS_%=:\n\t" \
  "s_memtime s[88:89]\n\t" \
  "s_memrealtime s[66:67]\n\t" \
  "s_waitcnt lgkmcnt(0)\n\t" \
  "s_cmp_eq_u32 s66, s86\n\t" \
  "s_cbranch_scc1 O_MS_%=\n\t" \
  "s_sub_u32 s60, s66, s83\n\t" \
  "s_lshl_b32 s60, s60, 4\n\t" \
  "s_sub_u32 s61, s88, s84\n\t" \
  "s_mul_i32 s61, s61, 11\n\t" \
  "s_lshr_b32 s61, s61, 4\n\t" \
  "s_sub_u32 s60, s60, s61\n\t" \
  "s_add_u32 s60, s60, 20\n\t" \
  "s_sub_u32 s60, s60, s80\n\t" \
  "s_min_i32 s60, s60, 64\n\t" \
  "s_max_i32 s60, s60, -96\n\t" \
  "s_ashr_i32 s60, s60, 2\n\t" \
  "s_add_u32 s80, s80, s60\n\t" \
  "s_add_u32 s60, s80, 8\n\t" \
  "s_lshr_b32 s60, s60, 4\n\t" \
  "s_cmp_eq_u32 s60, s59\n\t" \
  "s_cbranch_scc1 O_NEXT_%=\n\t" \
  "s_mov_b32 s59, s60\n\t" \
  "v_mov_b32 v58, s60\n\t" \
  "s_mov_b64 exec, 1\n\t" \
  "buffer_store_dword v58, off, s[24:27], s46 sc1\n\t" \
  "s_mov_b64 exec, -1\n\t" \
  "O_NEXT_%=:\n\t" \
  "s_add_u32 s57, s83, s82\n\t" \
  "s_sub_u32 s57, s57, 16\n\t" \
  "s_barrier\n\t" \
  "s_add_u32 s56, s56, 1\n\t" \
  "s_cmp_lt_u32 s56, 63\n\t" \
  "s_cbranch_scc1 O_LOOP_%=\n\t" \
  "s_branch O_END_%=\n\t" \
  "O_RETRY_%=:\n\t" \
  "s_cmp_lg_u32 s64, 0\n\t" \
  "s_cbranch_scc1 O_OK_%=\n\t" \
  "s_add_u32 s63, s63, 1\n\t" \
  "s_cmp_gt_u32 s63, 20000\n\t" \
  "s_cselect_b32 s64, 1, 0\n\t" \
  "s_nop 3\n\t" \
  "v_mov_b64 v[40:41], 0\n\t" \
  "v_mov_b64 v[42:43], 0\n\t" \
  "v_mov_b64 v[44:45], 0\n\t" \
  "v_mov_b64 v[46:47], 0\n\t" \
  "v_mov_b64 v[48:49], 0\n\t" \
  "v_mov_b64 v[50:51], 0\n\t" \
  "v_mov_b64 v[52:53], 0\n\t" \
  "v_mov_b64 v[54:55], 0\n\t" \
  "v_mov_b32 v72, 0\n\t" \
  "s_branch O_L1_%=\n\t" \
  "O_END_%=:\n\t" \
  "s_waitcnt vmcnt(0)\n\t" \
  "v_mov_b32 v100, s90\n\t" \
  "v_mov_b32 v101, s80\n\t" \
  "v_mov_b32 v102, s68\n\t" \
  "v_mov_b32 v103, s64\n\t" \
  "s_nop 7\n\t" \
  ""
#define O_ASM_CLOB "v8", "v9", "v10", "v11", "v12", "v13", "v14", "v15", "v16", "v17", "v18", "v19", "v20", "v21", "v22", "v23", "v24", "v25", "v26", "v27", "v28", "v29", "v30", "v31", "v32", "v33", "v34", "v35", "v36", "v37", "v38", "v39", "v40", "v41", "v42", "v43", "v44", "v45", "v46", "v47", "v48", "v49", "v50", "v51", "v52", "v53", "v54", "v55", "v56", "v57", "v58", "v59", "v60", "v61", "v62", "v63", "v64", "v65", "v66", "v67", "v68", "v69", "v70", "v71", "v72", "v73", "v74", "v75", "v76", "v77", "v78", "v79", "s56", "s57", "s58", "s59", "s60", "s61", "s62", "s63", "s64", "s65", "s66", "s67", "s68", "s69", "s70", "s71", "s72", "s73", "s74", "s75", "s76", "s77", "s78", "s79", "s80", "s81", "s82", "s83", "s84", "s85", "s86", "s87", "s88", "s89", "s90", "s91", "s92", "s93", "s94", "s95", "s96", "s97", "s98", "s99", "vcc", "scc", "memory"
#define TD_INIT 74

constexpr int S_ = 2048, D_ = 576, H_ = 32, M_ = 8, NSEG = 64, F_ = 4608, HE = 4096;
constexpr unsigned OOB = 0xF0000000u;
constexpr unsigned SPINCAP = 40000;
constexpr unsigned F_EPOCH = 0, F_TMO = 128, F_HR = 256  , F_PAR = 1024  , F_OWN = 2048  , F_LN1 = 3072  ,
                   F_KV = 4096, F_KM = F_KV + 32 * 128, F_QA = F_KM + 32 * 128, F_WO = F_QA + 32 * 128  , F_F1 = F_WO + 54 * 128  ,
                   F_Q = F_F1 + 36 * 128  , F_SLOT = F_Q + 16 * 128  ;
constexpr unsigned INIT_BYTES = F_SLOT + 256 * 64;
constexpr unsigned REG = 2u << 20;
constexpr unsigned SLOT = 11 * 16384;
constexpr unsigned W_STAMP = 2 * REG;
constexpr unsigned W_KVX = W_STAMP + 262144;
constexpr unsigned W_KVM = W_KVX + NSEG * H_ * 16384;
constexpr unsigned W_XN = W_KVM + NSEG * H_ * 8192;
constexpr unsigned W_H = W_XN + NSEG * 36864;
constexpr unsigned W_O = W_H + S_ * D_ * 2;
constexpr unsigned W_HID = W_O + NSEG * 262144;
constexpr unsigned W_Q = W_HID + NSEG * 294912;
constexpr unsigned W_TRACE = W_Q + NSEG * H_ * 8192;
constexpr unsigned W_END = W_TRACE + 80 * 4096 + 4096;
constexpr int U0 = 104;
#define T2DIV 2
#define CODEPF 1
#define WARM1 0
#define OWARM 32
#define AFTK 3
#define OUTAUX 0
#define T2WARM 5
#define DLY_QA 4
#define DLY_WO 6
#define DLY_F1 2
#define DLY_F2 8

constexpr int L_WV = 0, L_MEM = 90112, L_QK = L_MEM + 9216, QKROW = 320, QKM = 8 * QKROW + 32, L_LATE = L_QK + 2 * QKM,
              L_END = 147456, LDS_BYTES = L_END + 64;

__device__ __forceinline__ float bflo(unsigned v) { return __uint_as_float(v << 16); }
__device__ __forceinline__ float bfhi(unsigned v) { return __uint_as_float(v & 0xffff0000u); }
__device__ __forceinline__ unsigned pk2(float a, float b) { bf16x2 v = {(__bf16)a, (__bf16)b}; return __builtin_bit_cast(unsigned, v); }
__device__ __forceinline__ float rbf(float f) { return bflo(pk2(f, f)); }
__device__ __forceinline__ unsigned plo(unsigned r0, unsigned r1) { return __builtin_amdgcn_perm(r1, r0, 0x05040100u); }
__device__ __forceinline__ unsigned phi(unsigned r0, unsigned r1) { return __builtin_amdgcn_perm(r1, r0, 0x07060302u); }
__device__ __forceinline__ unsigned xcc_id() { return (unsigned)__builtin_amdgcn_s_getreg(20 | (3 << 11)) & 0xfu; }
#define RSRC(p, bytes) __builtin_amdgcn_make_buffer_rsrc((void*)(p), 0, (bytes), 0x00020000)
#define MFMA32(a, b, c) __builtin_amdgcn_mfma_f32_16x16x32_bf16(__builtin_bit_cast(bf16x8, a), __builtin_bit_cast(bf16x8, b), c, 0, 0, 0)
#define MFMA16(a, b, c) __builtin_amdgcn_mfma_f32_16x16x16bf16_1k(__builtin_bit_cast(s16x4, a), __builtin_bit_cast(s16x4, b), c, 0, 0, 0)
#define LD128(r, v, s) __builtin_amdgcn_raw_buffer_load_b128(r, v, s, 16)
#define LD64(r, v, s) __builtin_amdgcn_raw_buffer_load_b64(r, v, s, 16)
#define ST64(x, r, v, s) __builtin_amdgcn_raw_buffer_store_b64(x, r, v, s, 16)
#define LD32(r, v, s) __builtin_amdgcn_raw_buffer_load_b32(r, v, s, 16)
#define ST128(x, r, v, s) { const v4u st_ = (x); __builtin_amdgcn_raw_buffer_store_b128(st_, r, v, s, 16); asm volatile("s_nop 1" :: "v"(st_) : "memory"); }
#define ST32(x, r, v, s) __builtin_amdgcn_raw_buffer_store_b32(x, r, v, s, 16)
#define SCHED() __builtin_amdgcn_sched_barrier(0)
#define VMWAIT0() asm volatile("s_waitcnt vmcnt(0)" ::: "memory")
__device__ __forceinline__ void lds_barrier() { asm volatile("s_waitcnt lgkmcnt(0)\n\ts_barrier" ::: "memory"); }
__device__ __forceinline__ float xsw16(float x, bool mx) { unsigned u = __float_as_uint(x); auto r = __builtin_amdgcn_permlane16_swap(u, u, false, false); float a = __uint_as_float(r[0]), b = __uint_as_float(r[1]); return mx ? fmaxf(a, b) : a + b; }
__device__ __forceinline__ float xsw32(float x, bool mx) { unsigned u = __float_as_uint(x); auto r = __builtin_amdgcn_permlane32_swap(u, u, false, false); float a = __uint_as_float(r[0]), b = __uint_as_float(r[1]); return mx ? fmaxf(a, b) : a + b; }
template <int CTRL> __device__ __forceinline__ float dpp_add(float x) { return x + __builtin_bit_cast(float, __builtin_amdgcn_update_dpp(0, __builtin_bit_cast(int, x), CTRL, 0xf, 0xf, true)); }
__device__ __forceinline__ float wave_sum(float x) { x = dpp_add<0x121>(x); x = dpp_add<0x122>(x); x = dpp_add<0x124>(x); x = dpp_add<0x128>(x); return xsw32(xsw16(x, false), false); }

#define PDECL
#define PM(i)
#define PEND(id) {}
#define PRO(k) {}
struct KArgs {
  const u16 *x, *mem0, *Wq, *Wk, *Wv, *Wo, *w1, *b1, *w2, *b2;
  u16* out; char* wb;
};
struct Args : KArgs { unsigned tag; };
#define STF(val, voff, off) ST64(((v2u){(unsigned)(val), A.tag}), rw, voff, off)
__device__ __forceinline__ int flagval(v2u f, unsigned tag) { return f.y == tag ? (int)f.x : -1; }

template <int SLEEP>
__device__ __forceinline__ int wait_flags(const Args& A, rsrc_t rw, unsigned voff, unsigned soff, int thr) {
  int val;
  for (unsigned spins = 0;;) {
    asm volatile("" ::: "memory");
    val = flagval(LD64(rw, voff, soff), A.tag);
    if (__all(val >= thr)) break;
    __builtin_amdgcn_s_sleep(SLEEP);
    if (++spins > SPINCAP) { STF(1, 0, F_TMO); break; }
    if ((spins & 63u) == 0u && __builtin_amdgcn_readfirstlane(flagval(LD64(rw, 0, F_TMO), A.tag)) == 1) break;
  }
  asm volatile("" ::: "memory");
  return val;
}
__device__ __forceinline__ void raise_flag(const Args& A, rsrc_t rw, unsigned off, int val) {
  asm volatile("s_waitcnt vmcnt(0) lgkmcnt(0)\n\ts_barrier" ::: "memory");
  if (threadIdx.x == 0) STF(val, 0, off);
}
__device__ __forceinline__ void skew(int w) { for (int i = 0; i < w; ++i) asm volatile("s_nop 1"); }
template <int P>
__device__ __forceinline__ void dma(rsrc_t rw, unsigned l16, unsigned soff, unsigned ldsaddr) {
  asm volatile("s_mov_b32 m0, %3\n\ts_nop 0\n\tbuffer_load_dwordx4 %0, %1, %2 offen offset:%4 sc1 lds"
               :: "v"(l16), "s"(rw), "s"(soff + (P >> 2) * 4096), "s"(ldsaddr + (P >> 2) * 4096), "i"((P & 3) * 1024) : "memory");
}
#define DMAP(p, ...) { if ((p) == 0) dma<0>(__VA_ARGS__); if ((p) == 1) dma<1>(__VA_ARGS__); if ((p) == 2) dma<2>(__VA_ARGS__); if ((p) == 3) dma<3>(__VA_ARGS__); \
  if ((p) == 4) dma<4>(__VA_ARGS__); if ((p) == 5) dma<5>(__VA_ARGS__); if ((p) == 6) dma<6>(__VA_ARGS__); if ((p) == 7) dma<7>(__VA_ARGS__); if ((p) == 8) dma<8>(__VA_ARGS__); }
__device__ __forceinline__ void dma9(rsrc_t rw, unsigned l16, unsigned soff, unsigned ldsaddr) {
  asm volatile("s_nop 4");
#pragma unroll
  for (int p = 0; p < 9; ++p) DMAP(p, rw, l16, soff, ldsaddr)
}
__device__ __forceinline__ void warm(const u16* Wm, unsigned bytes, unsigned voff, unsigned so, unsigned step, int n) {
  const int w = __builtin_amdgcn_readfirstlane(threadIdx.x >> 6);
  auto r = RSRC(Wm, bytes);
  asm volatile("s_nop 4");
  for (int i = 0; i < n; ++i)
    asm volatile("s_mov_b32 m0, %3\n\ts_nop 0\n\tbuffer_load_dwordx4 %0, %1, %2 offen lds" :: "v"(voff), "s"(r), "s"(so + i * step), "s"((unsigned)(131072 + w * 1024)) : "memory");
}
__device__ __forceinline__ void delay_us(int us) { const unsigned t0 = (unsigned)__builtin_amdgcn_s_memrealtime(); while ((unsigned)__builtin_amdgcn_s_memrealtime() - t0 < 100u * us) __builtin_amdgcn_s_sleep(8); }

typedef short v4s __attribute__((ext_vector_type(4)));
__device__ __forceinline__ v2u trd(unsigned ldsaddr) {
  return __builtin_bit_cast(v2u, __builtin_amdgcn_ds_read_tr16_b64_v4i16((__attribute__((address_space(3))) v4s*)(uintptr_t)ldsaddr));
}
__device__ __forceinline__ void dma_unit(rsrc_t src, unsigned v0, unsigned v1, unsigned soff, unsigned step, unsigned ldsaddr) {
  unsigned t;
  asm volatile("s_mov_b32 m0, %6\n\ts_mov_b32 %0, %4\n\tbuffer_load_dwordx4 %1, %3, %0 offen lds\n\t"
               "s_addk_i32 m0, 0x400\n\ts_add_u32 %0, %0, %5\n\tbuffer_load_dwordx4 %2, %3, %0 offen lds\n\t"
               "s_addk_i32 m0, 0x400\n\ts_add_u32 %0, %0, %5\n\tbuffer_load_dwordx4 %1, %3, %0 offen lds\n\t"
               "s_addk_i32 m0, 0x400\n\ts_add_u32 %0, %0, %5\n\tbuffer_load_dwordx4 %2, %3, %0 offen lds"
               : "=&s"(t) : "v"(v0), "v"(v1), "s"(src), "s"(soff), "s"(step), "s"(ldsaddr) : "memory", "scc");
}
template <int... I, class F> __device__ __forceinline__ void sfor_(std::integer_sequence<int, I...>, F f) { (f(std::integral_constant<int, I>{}), ...); }
template <int NA_, int NB_, class SO, class CO>
__device__ __forceinline__ void stream2(rsrc_t srcA, rsrc_t srcB, unsigned v0, unsigned v1, unsigned ldsb, SO soff, CO consume) {
  constexpr int NU = NA_ + NB_;
  asm volatile("s_nop 4");
  sfor_(std::make_integer_sequence<int, NU + 8>{}, [&](auto uc) __attribute__((always_inline)) {
    constexpr int u = uc, c = u - 7;
    if constexpr (c >= 0 && c < NU) asm volatile("s_waitcnt vmcnt(%0)" :: "i"(4 * (NU - 1 - c < 6 ? NU - 1 - c : 6)) : "memory");
    if constexpr (c >= 1) { asm volatile("s_waitcnt lgkmcnt(0)" ::: "memory"); consume(std::integral_constant<int, (c >= 1 ? c - 1 : 0)>{}, 0, std::true_type{}); SCHED(); }
    if constexpr (c >= 0 && c < NU) { consume(std::integral_constant<int, (c >= 0 && c < NU ? c : 0)>{}, (c & 7) * 4096, std::false_type{}); SCHED(); }
    if constexpr (u < NU) {
      dma_unit(u < NA_ ? srcA : srcB, v0, v1, soff(u, 0), soff(0, 1) - soff(0, 0), ldsb + (u & 7) * 4096);
    }
  });
}
template <int NU, class SO, class CO>
__device__ __forceinline__ void stream(rsrc_t src, unsigned v0, unsigned v1, unsigned ldsb, SO soff, CO consume) { stream2<NU, 0>(src, src, v0, v1, ldsb, soff, consume); }
template <int NA>
__device__ __forceinline__ void load_slab64(v16u (&W)[18], const u16* Wm, int ldw, int colb, int w) {
  const int lane = threadIdx.x & 63, g = lane >> 4, q = (lane >> 2) & 3, p = lane & 3;
  const unsigned v = (lane >> 3) * ldw * 2 + ((lane & 7) ^ ((lane >> 4) & 1)) * 16, lb = w * 32768;
  const unsigned r0 = lb + (8 * g + q) * 128 + ((2 * p) ^ (q >> 1)) * 16, r1 = r0 ^ 16;
  stream<18>(RSRC(Wm, D_ * ldw * 2), v, v, lb, [&](int u, int i) { return (unsigned)(((32 * u + 8 * i) * ldw + colb) * 2); },
    [&](auto cc, int so, auto fin) __attribute__((always_inline)) {
      constexpr int c = cc; constexpr bool F = fin;
      if constexpr (!F) {
#pragma unroll
        for (int a = 0; a < 4; ++a)
#pragma unroll
          for (int t = 0; t < 2; ++t) { const v2u f = trd((a < 2 ? r0 : r1) + so + t * 512 + (a & 1) * 8); W[c][4 * a + 2 * t] = f.x; W[c][4 * a + 2 * t + 1] = f.y; }
      } else if constexpr (c < NA) asm volatile("" : "+a"(W[c])); else asm volatile("" : "+v"(W[c]));
    });
  asm volatile("s_barrier" ::: "memory");
}
#define FRAG(W, a) ((v4u){W[4 * (a)], W[4 * (a) + 1], W[4 * (a) + 2], W[4 * (a) + 3]})

__device__ __forceinline__ void ln_row(v4u a, v4u b, bool act, v4u& oa, v4u& ob) {
  float v[16];
#pragma unroll
  for (int i = 0; i < 4; ++i) { v[2 * i] = bflo(a[i]); v[2 * i + 1] = bfhi(a[i]); v[8 + 2 * i] = bflo(b[i]); v[9 + 2 * i] = bfhi(b[i]); }
  float s = 0;
#pragma unroll
  for (int i = 0; i < 16; ++i) s += v[i];
  const float mean = wave_sum(s) * (1.f / D_);
  float q = 0;
#pragma unroll
  for (int i = 0; i < 16; ++i) { const float d = v[i] - mean; q = fmaf(d, d, q); }
  q = wave_sum(act ? q : 0.f);
  const float mb = rbf(mean), den = rbf(sqrtf(rbf(rbf(q * (1.f / D_)) + 1e-5f))), inv = 1.f / den;
#pragma unroll
  for (int i = 0; i < 8; ++i) {
    const unsigned dd = pk2(v[2 * i] - mb, v[2 * i + 1] - mb);
    const unsigned o = pk2(bflo(dd) * inv, bfhi(dd) * inv);
    if (i < 4) oa[i] = o; else ob[i - 4] = o;
  }
}
__device__ __forceinline__ void ln_job(const Args& A, rsrc_t rw, char* lds, int j) {
  const int lane = threadIdx.x & 63, w = __builtin_amdgcn_readfirstlane(threadIdx.x >> 6);
  const bool act = lane < 36;
  const unsigned vin = act ? lane * 32 : OOB;
  auto rx = RSRC(A.x, S_ * D_ * 2);
  v4u a[4], b[4];
#pragma unroll
  for (int q = 0; q < 4; ++q) { const int row = j * 16 + 4 * q + w;
    a[q] = __builtin_amdgcn_raw_buffer_load_b128(rx, vin, row * (D_ * 2), 0); b[q] = __builtin_amdgcn_raw_buffer_load_b128(rx, vin + 16, row * (D_ * 2), 0); }
  SCHED();
#pragma unroll
  for (int q = 0; q < 4; ++q) { v4u oa, ob; ln_row(a[q], b[q], act, oa, ob);
    char* sw = lds + lane * 528 + (4 * q + w) * 16; *(v4u*)(sw) = oa; *(v4u*)(sw + 256) = ob; }
  lds_barrier();
  for (int k = w; k < 18; k += 4) ST128(*(const v4u*)(lds + (2 * k + (lane >> 5)) * 528 + (lane & 31) * 16), rw, lane * 16, W_XN + j * 18432 + k * 1024)
  raise_flag(A, rw, F_LN1 + 8 * j, 1);
}
__device__ __forceinline__ void wait_xn(const Args& A, rsrc_t rw, int lane, int half) { wait_flags<2>(A, rw, lane * 8, F_LN1 + half * 512, 1); }

template <bool ISK>
__device__ __forceinline__ void kv_wave(const Args& A, rsrc_t rw, char* lds, const u16* Wm, int colb, unsigned ob0, unsigned obs, unsigned ff, int w, int h) {
  const int lane = threadIdx.x & 63, n = lane & 15, g = lane >> 4; const unsigned l16 = lane * 16;
  v16u W[18];
  load_slab64<13>(W, Wm, HE, colb, w);
  PRO(0)
  wait_xn(A, rw, lane, 0);
  dma9(rw, l16, W_XN + w * 9216, w * 9216);
  VMWAIT0();
  PDECL
  for (int s = 0; s < NSEG; ++s) {
    asm volatile("s_waitcnt vmcnt(4)\n\ts_barrier" ::: "memory");
    PM(0)
    const int sn = min(s + 1, NSEG - 1);
    if (s == 8) wait_xn(A, rw, lane, 1);
    const unsigned dsrc = W_XN + sn * 36864 + w * 9216, ddst = ((s + 1) & 1) * 36864 + w * 9216;
    PM(1)
    skew(w);
    const char* xb = lds + (s & 1) * 36864 + l16;
    f32x4 acc[2][4] = {};
    v4u xf[36];
    xf[0] = *(const v4u*)(xb); xf[1] = *(const v4u*)(xb + 18432);
#pragma unroll
    for (int q = 0; q < 36; ++q) {
      const int ks = q >> 1, rt = q & 1;
      SCHED();
      if (q == 28) {
        asm volatile("s_waitcnt vmcnt(9)\n\ts_barrier" ::: "memory");
        if (w == 0) STF(s, lane == 0 ? 0u : OOB, ff);
        SCHED();
      }
#pragma unroll
      for (int a = 0; a < 4; ++a) {
        acc[rt][a] = ISK ? MFMA32(FRAG(W[ks], a), xf[q], acc[rt][a]) : MFMA32(xf[q], FRAG(W[ks], a), acc[rt][a]);
        if (a == 0 && q + 2 < 36) { SCHED(); xf[q + 2] = *(const v4u*)(xb + rt * 18432 + (ks + 1) * 1024); SCHED(); }
        if (a == 2 && q >= 3 && q < 12) { SCHED(); DMAP(q - 3, rw, l16, dsrc, ddst) SCHED(); }
      }
    }
    SCHED();
    PM(2)
    const unsigned ob = ob0 + s * obs;
#pragma unroll
    for (int q = 0; q < 4; ++q) {
      v4u o;
      if (ISK) { const int kt = q >> 1, i = 2 * (q & 1);
        o = (v4u){pk2(acc[kt][0][i], acc[kt][1][i]), pk2(acc[kt][2][i], acc[kt][3][i]), pk2(acc[kt][0][i + 1], acc[kt][1][i + 1]), pk2(acc[kt][2][i + 1], acc[kt][3][i + 1])};
        ST128(o, rw, l16, ob + (kt * 4 + 2 * (w & 1) + (q & 1)) * 1024);
      } else {
        o = (v4u){pk2(acc[0][q][0], acc[0][q][1]), pk2(acc[0][q][2], acc[0][q][3]), pk2(acc[1][q][0], acc[1][q][1]), pk2(acc[1][q][2], acc[1][q][3])};
        ST128(o, rw, l16, ob + 8192 + (4 * (w & 1) + q) * 1024);
      }
    }
    if (s < 5) raise_flag(A, rw, ff, s + 1);
    PM(3)
  }
  if (h >= 0) PEND(h)
  raise_flag(A, rw, ff, NSEG);
}

__device__ __forceinline__ void qa_role(const Args& A, rsrc_t rw, char* lds, int h) {
  const int lane = threadIdx.x & 63, n = lane & 15, g = lane >> 4; const unsigned l16 = lane * 16;
  const int w = __builtin_amdgcn_readfirstlane(threadIdx.x >> 6), rt = w & 1, wc = w >> 1;
  const unsigned fq = F_QA + h * 128, fv = lane == 0 ? F_KV + 128 * h : (lane == 1 ? F_Q + 128 * (h >> 1) : OOB);
  PRO(0)
  PDECL
  int pk = 0, kmk = 0;
  auto fetch = [&](int s) {
    if (pk <= s) { const int val = wait_flags<4>(A, rw, fv, 0, lane < 2 ? s + 1 : INT_MIN); pk = min(__builtin_amdgcn_readlane(val, 0), __builtin_amdgcn_readlane(val, 1)); }
    const unsigned kb = W_KVX + (s * H_ + h) * 16384 + w * 4096, qb = W_Q + (s * H_ + h) * 8192 + w * 2048, d = (s & 1) * 24576;
    asm volatile("s_nop 4");
    dma<0>(rw, l16, kb, d + w * 4096); dma<1>(rw, l16, kb, d + w * 4096); dma<2>(rw, l16, kb, d + w * 4096); dma<3>(rw, l16, kb, d + w * 4096);
    dma<0>(rw, l16, qb, d + 16384 + w * 2048); dma<1>(rw, l16, qb, d + 16384 + w * 2048);
  };
  fetch(0);
  asm volatile("s_waitcnt vmcnt(0)\n\ts_barrier" ::: "memory");
  for (int s = 0; s < NSEG; ++s) {
    if (s + 1 < NSEG) fetch(s + 1);
    PM(1)
    const char* sb = lds + (s & 1) * 24576 + l16;
    v4u qf[4], Kx[8], Vx[4]; f32x4 s0 = {}, s1 = {}, sm = {};
#pragma unroll
    for (int u = 0; u < 4; ++u) { qf[u] = *(const v4u*)(sb + 16384 + (rt * 4 + u) * 1024); Kx[u] = *(const v4u*)(sb + u * 1024); Kx[4 + u] = *(const v4u*)(sb + 4096 + u * 1024); }
#pragma unroll
    for (int a = 0; a < 4; ++a) Vx[a] = *(const v4u*)(sb + 8192 + (4 * wc + a) * 1024);
    SCHED();
#pragma unroll
    for (int u = 0; u < 4; ++u) { s0 = MFMA32(Kx[u], qf[u], s0); s1 = MFMA32(Kx[4 + u], qf[u], s1); }
    float z[10]; const float cs2 = 0.08838834764831845f * 1.4426950408889634f;
#pragma unroll
    for (int i = 0; i < 4; ++i) {
      const bool vis = 4 * g + i <= n;
      z[i] = (rt == 1 || vis) ? s0[i] * cs2 : -1.4426950408889634e9f; z[4 + i] = (rt == 1 && vis) ? s1[i] * cs2 : -1.4426950408889634e9f;
    }
    float mx = fmaxf(fmaxf(fmaxf(z[0], z[1]), fmaxf(z[2], z[3])), fmaxf(fmaxf(z[4], z[5]), fmaxf(z[6], z[7])));
    asm volatile("" :: "v"(mx));
    PM(4)
    if (kmk <= s) kmk = __builtin_amdgcn_readfirstlane(wait_flags<1>(A, rw, 0, F_KM + 128 * h, s + 1));
    PM(5)
    if (s == 63) PRO(2)
    const unsigned mb = W_KVM + (s * H_ + h) * 8192;
    v4u kf[4];
#pragma unroll
    for (int u = 0; u < 4; ++u) kf[u] = LD128(rw, l16 + u * 1024, mb);
    const v2u vm0 = LD64(rw, lane * 8, mb + 4096 + wc * 1024), vm1 = LD64(rw, lane * 8 + 512, mb + 4096 + wc * 1024);
    SCHED();
#pragma unroll
    for (int u = 0; u < 4; ++u) sm = MFMA32(kf[u], qf[u], sm);
    z[8] = sm[0] * cs2; z[9] = sm[1] * cs2;
    mx = fmaxf(mx, fmaxf(z[8], z[9]));
    mx = xsw32(xsw16(mx, true), true);
    float sum = 0;
#pragma unroll
    for (int i = 0; i < 10; ++i) { z[i] = __builtin_amdgcn_exp2f(z[i] - mx); sum += z[i]; }
    const v4u px = {pk2(z[0], z[1]), pk2(z[2], z[3]), pk2(z[4], z[5]), pk2(z[6], z[7])};
    const v4u pm = {pk2(z[8], z[9]), 0u, 0u, 0u};
    f32x4 o[4];
#pragma unroll
    for (int a = 0; a < 4; ++a) o[a] = MFMA32(Vx[a], px, ((f32x4){0, 0, 0, 0}));
#pragma unroll
    for (int a = 0; a < 4; ++a) { const v4u va = {a < 2 ? vm0[a & 1] : vm1[a & 1], 0u, 0u, 0u}; o[a] = MFMA32(va, pm, o[a]); }
    const float inv = __builtin_amdgcn_rcpf(xsw32(xsw16(sum, false), false));
#pragma unroll
    for (int ih = 0; ih < 2; ++ih) { const f32x4 p0 = o[2 * ih] * inv, p1 = o[2 * ih + 1] * inv;
      const v4u ov = {pk2(p0[0], p0[1]), pk2(p0[2], p0[3]), pk2(p1[0], p1[1]), pk2(p1[2], p1[3])};
      ST128(ov, rw, l16, W_O + (s * 2 + rt) * 131072 + (4 * h + 2 * wc + ih) * 1024); }
    if (s == 62) PRO(1)
    if (s == 63) PRO(3)
    PM(6)
    raise_flag(A, rw, fq, s + 1);
    PM(0)
  }
  PEND(32 + h)
}

__device__ __forceinline__ void f1_role(const Args& A, rsrc_t rw, char* lds, int j, int rt) {
  const int lane = threadIdx.x & 63, n = lane & 15, g = lane >> 4; const unsigned l16 = lane * 16;
  const int w = __builtin_amdgcn_readfirstlane(threadIdx.x >> 6);
  v16u W[18];
  load_slab64<14>(W, A.w1, F_, j * 256 + 64 * w, w);
  PRO(0)
  const v4u ones = {0x3F803F80u, 0x3F803F80u, 0x3F803F80u, 0x3F803F80u};
  f32x4 bias[4], csum[4] = {};
  { auto rb = RSRC(A.b1, F_ * 2); const unsigned vo = (j * 256 + 64 * w + 16 * g) * 2;
    const v4u a = __builtin_amdgcn_raw_buffer_load_b128(rb, vo, 0, 0), b = __builtin_amdgcn_raw_buffer_load_b128(rb, vo + 16, 0, 0);
    bias[0] = (f32x4){bflo(a[0]), bfhi(a[0]), bflo(a[1]), bfhi(a[1])}; bias[1] = (f32x4){bflo(a[2]), bfhi(a[2]), bflo(a[3]), bfhi(a[3])};
    bias[2] = (f32x4){bflo(b[0]), bfhi(b[0]), bflo(b[1]), bfhi(b[1])}; bias[3] = (f32x4){bflo(b[2]), bfhi(b[2]), bflo(b[3]), bfhi(b[3])}; }
#pragma unroll
  for (int ks = 0; ks < 18; ++ks)
#pragma unroll
    for (int a = 0; a < 4; ++a) csum[a] = MFMA32(FRAG(W[ks], a), ones, csum[a]);
#pragma unroll
  for (int a = 0; a < 4; ++a) asm volatile("" : "+v"(csum[a]), "+v"(bias[a]));
  const unsigned ff = F_F1 + (rt * 18 + j) * 128, fv = (lane < 18 && w == 0) ? lane * 128 : OOB;
  const unsigned v5 = w < 2 ? l16 : OOB;
  float* const stat = (float*)(lds + 40960);
  bool have = false;
  auto fetch = [&](int u, int buf) {
    const unsigned src = W_H + u * 18432, dst = buf * 20480;
    asm volatile("s_nop 4");
    dma<0>(rw, l16, src + w * 4096, dst + w * 4096); dma<1>(rw, l16, src + w * 4096, dst + w * 4096); dma<2>(rw, l16, src + w * 4096, dst + w * 4096);
    dma<3>(rw, l16, src + w * 4096, dst + w * 4096); dma<0>(rw, v5, src + 16384 + w * 1024, dst + 16384 + w * 1024);
  };
  PDECL
  for (int s = 0; s < NSEG; ++s) {
    const int u = 2 * s + rt, grp = u < U0 ? rt : (u - U0) % 3, grn = u + 2 < U0 ? rt : (u + 2 - U0) % 3;
    const bool pend = have;
    if (!have) {
      if (w == 0) wait_flags<1>(A, rw, fv, F_WO + grp * 2304, lane < 18 ? u + 1 : INT_MIN);
      asm volatile("s_barrier" ::: "memory");
      PM(0)
      if (s == 63) PRO(2)
      fetch(u, s & 1);
      VMWAIT0();
    } else asm volatile("s_waitcnt vmcnt(2)" ::: "memory");
    const char* xb = lds + (s & 1) * 20480 + l16;
    f32x4 s1 = {}, s2 = {};
    { v4u xo[5];
#pragma unroll
      for (int k = 0; k < 5; ++k) xo[k] = *(const v4u*)(xb + (k < 4 ? w * 4096 + k * 1024 : 16384 + w * 1024));
      asm volatile("s_barrier" ::: "memory");
#pragma unroll
      for (int k = 0; k < 5; ++k) { s1 = MFMA32(ones, xo[k], s1); s2 = MFMA32(xo[k], xo[k], s2); } }
    v2u fl = {0, 0};
    PM(1)
    f32x4 acc[4] = {};
    float mean_b = 0, inv = 0; int hv = 0; f32x4 pa = {}, pb = {};
    const unsigned nsrc = W_H + (u + 2) * 18432, ndst = ((s + 1) & 1) * 20480;
    v4u xf[18];
    xf[0] = *(const v4u*)(xb); xf[1] = *(const v4u*)(xb + 1024);
#pragma unroll
    for (int ks = 0; ks < 18; ++ks) {
      SCHED();
      if (ks == 2) {
        const float d = (n & 2) ? ((n & 1) ? s2[3] : s2[2]) : ((n & 1) ? s2[1] : s2[0]);
        if (g == (n >> 2)) *(f32x2*)(stat + (n * 4 + w) * 2) = (f32x2){s1[0], d};
        SCHED();
      }
      if (ks == 1) { fl = LD64(rw, fv, F_WO + grn * 2304); SCHED(); }
      if (ks == 11) { asm volatile("" : "+v"(fl)); const bool rdy = __all(lane >= 18 || flagval(fl, A.tag) > u + 2); if (w == 0) ((int*)stat)[128] = rdy; SCHED(); }
      if (ks == 12) {
        asm volatile("s_waitcnt vmcnt(0) lgkmcnt(0)\n\ts_barrier" ::: "memory");
        hv = ((int*)stat)[128]; pa = *(const f32x4*)(stat + n * 8); pb = *(const f32x4*)(stat + n * 8 + 4);
        if (pend && w == 0) STF(s, lane == 0 ? 0u : OOB, ff);
        SCHED();
      }
      if (ks == 13) { have = __builtin_amdgcn_readfirstlane(hv); SCHED(); }
#pragma unroll
      for (int a = 0; a < 4; ++a) {
        acc[a] = MFMA32(FRAG(W[ks], a), xf[ks], acc[a]);
        if (a == 0 && ks + 2 < 18) { SCHED(); xf[ks + 2] = *(const v4u*)(xb + (ks + 2) * 1024); SCHED(); }
        if (a == 2 && ks >= 13) { SCHED(); if (have) { if (ks < 17) DMAP(ks - 13, rw, l16, nsrc + w * 4096, ndst + w * 4096) else dma<0>(rw, v5, nsrc + 16384 + w * 1024, ndst + 16384 + w * 1024); } SCHED(); }
      }
    }
    {
      const float mean = ((pa[0] + pa[2]) + (pb[0] + pb[2])) * (1.f / D_), var = fmaxf(fmaf(-mean, mean, ((pa[1] + pa[3]) + (pb[1] + pb[3])) * (1.f / D_)), 0.f);
      mean_b = -rbf(mean); inv = __builtin_amdgcn_rcpf(rbf(__builtin_amdgcn_sqrtf(rbf(rbf(var) + 1e-5f))));
    }
    SCHED();
    PM(2)
#pragma unroll
    for (int ih = 0; ih < 2; ++ih) {
      f32x2 v[4], t[4];
#pragma unroll
      for (int e = 0; e < 4; ++e) { const int a = 2 * ih + (e >> 1), i = 2 * (e & 1);
        v[e] = __builtin_elementwise_fma((f32x2){csum[a][i], csum[a][i + 1]}, (f32x2){mean_b, mean_b}, (f32x2){acc[a][i], acc[a][i + 1]}); }
      SCHED();
#pragma unroll
      for (int e = 0; e < 4; ++e) { const int a = 2 * ih + (e >> 1), i = 2 * (e & 1); v[e] = __builtin_elementwise_fma(v[e], (f32x2){inv, inv}, (f32x2){bias[a][i], bias[a][i + 1]}); }
      SCHED();
#pragma unroll
      for (int e = 0; e < 4; ++e) t[e] = v[e] * v[e];
      SCHED();
#pragma unroll
      for (int e = 0; e < 4; ++e) t[e] = __builtin_elementwise_fma(t[e], (f32x2){-0.044715f * 2.302208198f, -0.044715f * 2.302208198f}, (f32x2){-2.302208198f, -2.302208198f});
      SCHED();
#pragma unroll
      for (int e = 0; e < 4; ++e) t[e] = t[e] * v[e];
      SCHED();
#pragma unroll
      for (int e = 0; e < 4; ++e) t[e] = (f32x2){__builtin_amdgcn_exp2f(t[e][0]), __builtin_amdgcn_exp2f(t[e][1])};
      SCHED();
#pragma unroll
      for (int e = 0; e < 4; ++e) t[e] = t[e] + (f32x2){1.f, 1.f};
      SCHED();
#pragma unroll
      for (int e = 0; e < 4; ++e) t[e] = (f32x2){__builtin_amdgcn_rcpf(t[e][0]), __builtin_amdgcn_rcpf(t[e][1])};
      SCHED();
#pragma unroll
      for (int e = 0; e < 4; ++e) t[e] = t[e] * v[e];
      SCHED();
      const v4u ov = {pk2(t[0][0], t[0][1]), pk2(t[1][0], t[1][1]), pk2(t[2][0], t[2][1]), pk2(t[3][0], t[3][1])};
      ST128(ov, rw, l16, W_HID + u * 147456 + (8 * j + 2 * w + ih) * 1024)
      SCHED();
    }
    if (s == 62) PRO(1)
    if (s == 63) PRO(3)
    PM(3)
    if (!have) raise_flag(A, rw, ff, s + 1);
    PM(4)
  }
  PEND(64 + rt * 18 + j)
}

template <bool ISF2>
__device__ __forceinline__ void t2_role(const Args& A, rsrc_t rw, char* lds, int cs, int grp) {
  constexpr int NK = ISF2 ? 36 : 32, NP = ISF2 ? 5 : 8;
  const int lane = threadIdx.x & 63, n = lane & 15, g = lane >> 4; const unsigned l16 = lane * 16;
  const int w = __builtin_amdgcn_readfirstlane(threadIdx.x >> 6);
  v16u W[NK / 2];
  {
    const int q = (lane >> 2) & 3, p = lane & 3;
    const unsigned v0 = (lane >> 2) * (D_ * 2) + (lane & 3) * 16, lb = w * 32768;
    const unsigned r0 = lb + (16 * g + q) * 64 + ((p >> 1) ^ (2 * (g & 1))) * 16 + (p & 1) * 8, r1 = r0 ^ 32;
    if (grp < 2) warm(ISF2 ? A.w2 : A.Wo, 4 * NK * 32 * D_ * 2, (lane >> 3) * (D_ * 2) + (lane & 7) * 16, ((32 * NK * w + 8 * (cs & 1)) * D_ + 64 * (cs >> 1)) * 2, 16 * D_ * 2, T2WARM * NK / 4);
    stream<NK / 2>(RSRC(ISF2 ? A.w2 : A.Wo, 4 * NK * 32 * D_ * 2), v0, (lane >> 2) * (D_ * 2) + ((lane & 3) ^ 2) * 16, lb,
      [&](int u, int i) { return (unsigned)(((64 * (w * (NK / 2) + u) + 16 * i) * D_ + 32 * cs) * 2); },
      [&](auto cc, int so, auto fin) __attribute__((always_inline)) {
      constexpr int c = cc; constexpr bool F = fin;
        if constexpr (!F) {
#pragma unroll
          for (int e = 0; e < 8; ++e) { const v2u f = trd(((e & 2) ? r1 : r0) + so + (e >> 2) * 512 + (e & 1) * 256); W[c][2 * e] = f.x; W[c][2 * e + 1] = f.y; }
        } else if constexpr (c < 15) asm volatile("" : "+a"(W[c])); else asm volatile("" : "+v"(W[c]));
      });
    asm volatile("s_barrier" ::: "memory");
  }
  PRO(0)
  const int er = ISF2 ? 4 * w + g : lane >> 2, ec = ISF2 ? 2 * n : 8 * w + 2 * (lane & 3);
  const unsigned vrow = (er * D_ + 32 * cs + ec) * 2, vfrag = ((ec >> 3) * 16 + er) * 16 + (ec & 7) * 2;
  float b0 = 0, b1 = 0;
  if (ISF2) { auto rb = RSRC(A.b2, D_ * 2); const unsigned t = __builtin_amdgcn_raw_buffer_load_b32(rb, (32 * cs + ec) * 2, 0, 0); b0 = bflo(t); b1 = bfhi(t); }
  auto rx = RSRC(A.x, S_ * D_ * 2); auto ro = RSRC(A.out, S_ * D_ * 2);
  const unsigned fv = lane < NP ? lane * 128 : OOB, fs = ISF2 ? F_F1 + ((9 * w) >> 1) * 128 : F_QA + w * 1024, fw = F_WO + (grp * 18 + cs) * 128;
  constexpr int DP = NK / T2DIV;
  const unsigned ab0 = (ISF2 ? W_HID : W_O) + w * NK * 1024;
  v4u x[DP];
  bool have = false, pend = false;
  PDECL
  int up = 0, par = 0;
  for (int u = grp < 2 ? grp : U0 + 2; u < 2 * NSEG; par ^= 1) {
    const int un = u + 2 < U0 ? u + 2 : (u < U0 ? U0 + grp : u + 3), s = u >> 1;
    const unsigned ab = ab0 + u * (4 * NK * 1024), abn = ab0 + un * (4 * NK * 1024);
    if (!have) {
      wait_flags<1>(A, rw, fv, fs + (ISF2 ? (u & 1) * 2304 : 0), lane < NP ? s + 1 : INT_MIN);
      PM(0)
      if (s == 63) PRO(2)
#pragma unroll
      for (int k = 0; k < DP; ++k) { x[k] = LD128(rw, l16 + (k & 3) * 1024, ab + (k >> 2) * 4096); asm volatile("s_nop 7"); SCHED(); }
    }
    const unsigned rowb = u * 16 * (D_ * 2), hb = W_H + (u * 18 + cs) * 1024;
    const unsigned res = ISF2 ? LD32(rw, vfrag, hb) : __builtin_amdgcn_raw_buffer_load_b32(rx, vrow, rowb, 0);
    v2u fl = {0, 0};
    PM(1)
    SCHED();
    f32x4 acc[2] = {};
#define T2MM(k) { acc[0] = MFMA32(x[(k) % DP], FRAG(W[(k) >> 1], 2 * ((k) & 1)), acc[0]); acc[1] = MFMA32(x[(k) % DP], FRAG(W[(k) >> 1], 2 * ((k) & 1) + 1), acc[1]); SCHED(); }
#pragma unroll
    for (int k = 0; k < NK - DP; ++k) {
      if (k == NK - 2 * DP) { fl = LD64(rw, fv, fs + (ISF2 ? (un & 1) * 2304 : 0)); SCHED(); }
      T2MM(k)
      x[k % DP] = LD128(rw, l16 + ((k + DP) & 3) * 1024, ab + ((k + DP) >> 2) * 4096);
      SCHED();
      if (!ISF2 && k == 7 && pend) {
        asm volatile("s_waitcnt vmcnt(9)\n\ts_barrier" ::: "memory");
        if (w == 0) STF(up, lane == 0 ? 0u : OOB, fw);
        SCHED();
      }
    }
    asm volatile("" : "+v"(fl));
    have = __all(lane >= NP || flagval(fl, A.tag) > (un >> 1));
    if (have) {
#pragma unroll
      for (int k = NK - DP; k < NK; ++k) { T2MM(k) x[k % DP] = LD128(rw, l16 + ((k % DP) & 3) * 1024, abn + ((k % DP) >> 2) * 4096); SCHED(); }
    } else {
#pragma unroll
      for (int k = NK - DP; k < NK; ++k) T2MM(k)
    }
    PM(2)
    char* rb = lds + par * 9216;
#pragma unroll
    for (int i = 0; i < 4; ++i) { float* pw = (float*)(rb + (w * 16 + 4 * g + i) * 144 + n * 4); pw[0] = acc[0][i]; pw[16] = acc[1][i]; }
    if (!ISF2) *(int*)(lds + 18432 + par * 16 + w * 4) = have;
    lds_barrier();
    if (!ISF2) { const v4u hv = *(const v4u*)(lds + 18432 + par * 16); pend = __builtin_amdgcn_readfirstlane(hv[0] & hv[1] & hv[2] & hv[3]); }
    f32x2 t = {0, 0};
#pragma unroll
    for (int q = 0; q < 4; ++q) t += *(const f32x2*)(rb + (q * 16 + er) * 144 + ec * 4);
    float y0 = rbf(t[0]), y1 = rbf(t[1]);
    if (ISF2) { y0 = rbf(y0 + b0); y1 = rbf(y1 + b1); }
    const unsigned o = pk2(bflo(res) + y0, bfhi(res) + y1);
    PM(3)
    if (s == 62) PRO(1)
    if (s == 63) PRO(3)
    if (ISF2) __builtin_amdgcn_raw_buffer_store_b32(o, ro, vrow, rowb, OUTAUX);
    else { ST32(o, rw, vfrag, hb); if (!pend) raise_flag(A, rw, fw, u + 1); }
    PM(4)
    up = u + 1; u = un;
  }
  if (grp < 2) PEND((ISF2 ? 160 : 112) + grp * 18 + cs)
}

__device__ __forceinline__ unsigned comm_base(const Args& A) { return (unsigned)((((uintptr_t)A.wb + INIT_BYTES + REG - 1) & ~(uintptr_t)(REG - 1)) - (uintptr_t)A.wb); }
__device__ __forceinline__ void calibrate(const Args& A, rsrc_t rw, int id) {
  const int lane = threadIdx.x & 63, w = __builtin_amdgcn_readfirstlane(threadIdx.x >> 6);
  unsigned best = ~0u;
  for (int rep = 0; rep < 6; ++rep) {
    unsigned off = lane < 16 ? comm_base(A) + 4 * SLOT + (id & 1) * 16384 + w * 4096 + id * 256 + lane * 4 : OOB; asm volatile("" : "+v"(off));
    unsigned long long t0, t1; asm volatile("s_memtime %0\n\ts_waitcnt lgkmcnt(0)" : "=s"(t0) :: "memory");
    ST32(0u, rw, off, 0);
    asm volatile("s_waitcnt vmcnt(0)\n\ts_memtime %0\n\ts_waitcnt lgkmcnt(0)" : "=s"(t1) :: "memory");
    best = min(best, (unsigned)(t1 - t0));
  }
  const v2u e = {lane == 0 ? best & 0x7fffffffu : (unsigned)__builtin_amdgcn_s_memrealtime(), A.tag};
  ST64(e, rw, lane < 2 ? lane * 8 : OOB, F_PAR + (id * 4 + w) * 16);
}
__device__ __forceinline__ v2u get_par(const Args& A, rsrc_t rw) {
  const int lane = threadIdx.x & 63;
  v4u p;
  for (unsigned spins = 0;;) {
    asm volatile("" ::: "memory");
    p = LD128(rw, min(lane, 47) * 16, F_PAR);
    if (__all(p[1] == A.tag && p[3] == A.tag)) break;
    __builtin_amdgcn_s_sleep(2);
    if (++spins > SPINCAP) { STF(1, 0, F_TMO); break; }
  }
  unsigned key = (p[0] << 1) | ((min(lane, 47) >> 1) & 1);
#pragma unroll
  for (int o = 32; o; o >>= 1) key = min(key, (unsigned)__shfl_xor((int)key, o));
  return (v2u){(unsigned)__builtin_amdgcn_readfirstlane((key & 1u) ^ 1u), (unsigned)__builtin_amdgcn_readfirstlane(p[2])};
}
#define PINA(t, W) { if (t == 0) asm volatile("" : "+{a[0:31]}"(W[0])); if (t == 1) asm volatile("" : "+{a[32:63]}"(W[1])); if (t == 2) asm volatile("" : "+{a[64:95]}"(W[2])); \
  if (t == 3) asm volatile("" : "+{a[96:127]}"(W[3])); if (t == 4) asm volatile("" : "+{a[128:159]}"(W[4])); if (t == 5) asm volatile("" : "+{a[160:191]}"(W[5])); \
  if (t == 6) asm volatile("" : "+{a[192:223]}"(W[6])); if (t == 7) asm volatile("" : "+{a[224:255]}"(W[7])); }
#define AIN(W) "{a[0:31]}"(W[0]), "{a[32:63]}"(W[1]), "{a[64:95]}"(W[2]), "{a[96:127]}"(W[3]), "{a[128:159]}"(W[4]), "{a[160:191]}"(W[5]), "{a[192:223]}"(W[6]), "{a[224:255]}"(W[7])
typedef unsigned v8u __attribute__((ext_vector_type(8)));

__device__ __forceinline__ void head_role(const Args& A, rsrc_t rw, char* lds, int h, int copy) {
  const int tid = threadIdx.x, lane = tid & 63, n = lane & 15, g = lane >> 4, q = (lane >> 2) & 3, p = lane & 3;
  const int w = __builtin_amdgcn_readfirstlane(tid >> 6);
  const unsigned l16 = lane * 16;
  const int lrow = 16 * (g >> 1) + 4 * (g & 1) + 8 * (q >> 1) + (q & 1);
  const unsigned osv = (n >> 2) * 65536 + (g >> 1) * 256 + ((n >> 1) & 1) * 64 + (g & 1) * 32 + (n & 1) * 16 + (n < 8 ? 0u : OOB);
  const unsigned anv = (w == 0 && lane < 8) ? (lane >> 1) * 1024 + h * 32 + (lane & 1) * 16 : OOB;
  v2u pt; unsigned cb, oss;
  v32u Wqk[9]; v8u Wv[18];
  {
    const unsigned v0 = (lane >> 3) * (HE * 2) + (lane & 7) * 16, lb = w * 32768;
    const unsigned r0 = lb + lrow * 128 + ((2 * p) ^ (q >> 1)) * 16, r1 = r0 ^ 16, rv = r0 ^ ((w & 1) * 16);
    auto rq = RSRC((w < 2) ? A.Wq : A.Wk, D_ * HE * 2); auto rvv = RSRC(A.Wv, D_ * HE * 2);
    stream2<18, 18>(rq, rvv, v0, v0 ^ 16, lb,
      [&](int u, int i) { const int k = u % 18, t = (k >> 1) < 8 ? (k >> 1) ^ (2 * w) : 8;
                          return (unsigned)(((64 * t + 32 * (k & 1) + 8 * i) * HE + h * 128 + 64 * (u < 18 ? w & 1 : w >> 1)) * 2); },
      [&](auto cc, int so, auto fin) __attribute__((always_inline)) {
      constexpr int c = cc; constexpr bool F = fin;
        if constexpr (F) {
          if constexpr (c < 18 && (c & 1)) PINA(c >> 1, Wqk)
          if constexpr (c >= 18 && (c & 1)) asm volatile("" : "+v"(Wv[c >= 19 ? c - 19 : 0]), "+v"(Wv[c >= 18 ? c - 18 : 0]));
        } else if constexpr (c < 18) {
#pragma unroll
          for (int e = 0; e < 8; ++e) { const int a = e >> 1, b = e & 1; const v2u f = trd((a < 2 ? r0 : r1) + so + b * 256 + (a & 1) * 8);
            Wqk[c >> 1][8 * a + 4 * (c & 1) + 2 * b] = f.x; Wqk[c >> 1][8 * a + 4 * (c & 1) + 2 * b + 1] = f.y; }
        } else {
          constexpr int k = c - 18;
#pragma unroll
          for (int e = 0; e < 4; ++e) { const int a = e >> 1, b = e & 1; const v2u f = trd(rv + so + b * 256 + a * 8);
            Wv[(k & ~1) + a][4 * (k & 1) + 2 * b] = f.x; Wv[(k & ~1) + a][4 * (k & 1) + 2 * b + 1] = f.y; }
        }
        if constexpr (F && c == 14) {
          PRO(0)
          pt = get_par(A, rw); cb = comm_base(A) + pt.x * 8192u;
          oss = cb + (h >> 3) * 16384 + (h & 7) * 1024 + (w & 1) * 512 + (w >> 1) * 128;
#pragma unroll
          for (int k = 0; k < 4; ++k) { ST128(((v4u){~0u, ~0u, ~0u, ~0u}), rw, osv, oss + k * SLOT) ST32(pt.y - 1u, rw, w == 0 ? anv : OOB, cb + 10 * 16384 + 4096 + k * SLOT); }
        }
        if constexpr (F && c == 23) { asm volatile("s_barrier" ::: "memory"); STF(1, tid == 0 ? 0u : OOB, F_HR + 8 * h); }
      });
    asm volatile("s_barrier" ::: "memory");
  }
#pragma unroll
  for (int i = 0; i < 11; ++i) { const v8u f = Wv[i < 9 ? i : i + 7]; char* b = lds + L_WV + (w * 11 + i) * 2048 + l16;
    *(v4u*)(b) = (v4u){f[0], f[1], f[2], f[3]}; *(v4u*)(b + 1024) = (v4u){f[4], f[5], f[6], f[7]}; }
  PRO(1)
  const int half = (n >> 1) & 1, rown = 2 * (n >> 2) + (n & 1), r8 = n & 7;
  v16u L, SC;
  L[0] = l16; L[1] = half ? 0xEEEE : 0x4444;
  L[2] = L_QK + (w >> 1) * QKM + (2 * g) * QKROW + (g >> 1) * 16 + (64 * (w & 1) + 4 * n) * 2;
  L[3] = L_QK + r8 * QKROW + (r8 >> 2) * 16 + g * 32;
  L[4] = L_QK + QKM + rown * QKROW + (rown >> 2) * 16 + g * 32;
  L[5] = L_WV + w * 11 * 2048 + l16; L[6] = L_MEM + 8192 + l16;
#pragma unroll
  for (int k = 0; k < 4; ++k) L[7 + k] = L_MEM + ((2 * k) ^ (2 * w)) * 1024 + l16;
  L[11] = L_MEM + 8192 + w * 256 + lane * 4; L[12] = lane * 4;
  L[13] = osv;
  L[14] = L[4] + (w >> 1) * 128 + (w & 1) * 16;
  L[15] = anv;
  SC[0] = oss;
  SC[1] = cb + (8 + copy) * 16384 + w * 2048; SC[2] = cb + 10 * 16384 + copy * 1024 + w * 256;
  SC[3] = W_KVX + h * 16384 + w * 4096; SC[4] = W_KVM + h * 8192 + w * 1024; SC[5] = W_KVM + h * 8192 + 4096 + w * 512;
  SC[6] = cb + 10 * 16384 + 4096; SC[7] = F_KV + 128 * h; SC[8] = pt.y; SC[9] = (w >= 2 ? 1 : 0) | (w == 0 ? 2 : 0) | (w << 11);
  SC[10] = F_KM + 128 * h; SC[11] = W_TRACE + (h * 4 + w) * 1024; SC[12] = copy * 1024; SC[13] = F_OWN; SC[14] = L_LATE + 1024 + w * 4096; SC[15] = A.tag;
#pragma unroll
  for (int i = 0; i < 16; ++i) SC[i] = __builtin_amdgcn_readfirstlane(SC[i]);
  *(unsigned*)(lds + L_LATE + tid * 4) = pt.y;
  PRO(2)
  wait_flags<1>(A, rw, lane < 36 ? lane * 16 : OOB, F_OWN, lane < 36 ? 1 : INT_MIN);
  wait_flags<1>(A, rw, (lane & 31) * 8, F_HR, 1);
  wait_flags<1>(A, rw, 0, F_KV + 128 * h, 2);
  asm volatile("s_waitcnt vmcnt(0) lgkmcnt(0)\n\ts_barrier" ::: "memory");
  PRO(3)
  v4u res;
  asm volatile("s_nop 4\n\t" A_ASM_TEXT "\nkcode_end:"
               : "={v[100:103]}"(res)
               : AIN(Wqk), "{v[32:63]}"(Wqk[8]), "{v[196:203]}"(Wv[9]), "{v[204:211]}"(Wv[10]), "{v[212:219]}"(Wv[11]), "{v[220:227]}"(Wv[12]),
                 "{v[228:235]}"(Wv[13]), "{v[236:243]}"(Wv[14]), "{v[244:251]}"(Wv[15]), "{v[0:15]}"(L), "{s[40:55]}"(SC), "{s[24:27]}"(rw)
               : A_ASM_CLOB);
  if (res[3]) STF(1, 0, F_TMO);
}

__device__ __forceinline__ void own_role(const Args& A, rsrc_t rw, char* lds, int cs, int rho, int copy) {
  const int tid = threadIdx.x, lane = tid & 63, n = lane & 15, g = lane >> 4;
  const int w = __builtin_amdgcn_readfirstlane(tid >> 6);
  const int rl = (lane >> 3) & 3, cq = lane & 7;
  const unsigned msv = (lane >> 5) * (cs < 16 ? 16384 : 1024) + (cq >> 2) * 256 + (4 * (rl >> 1) + 2 * ((cq >> 1) & 1) + (rl & 1)) * 16 + (cq & 1) * 8;
  v2u pt; unsigned cb, mss;
  const v2u m0v = __builtin_amdgcn_raw_buffer_load_b64(RSRC(A.mem0, M_ * D_ * 2), ((4 * rho + rl) * D_ + 32 * cs + 4 * cq) * 2, 0, 0);
  v32u W[8];
  {
    const int q = (lane >> 2) & 3, p = lane & 3, lr = lane >> 2, gr = (lr & 9) | ((lr & 2) << 1) | ((lr & 4) >> 1);
    const unsigned lb = w * 32768;
    const unsigned r0 = lb + (32 * (g >> 1) + 16 * (q >> 1) + 2 * (g & 1) + (q & 1)) * 64 + ((p >> 1) ^ (2 * (q >> 1))) * 16 + (p & 1) * 8, r1 = r0 ^ 32;
    warm(A.Wo, HE * D_ * 2, (lane >> 3) * (D_ * 2) + (lane & 7) * 16, ((1024 * w + 8 * (cs & 1)) * D_ + 64 * (cs >> 1)) * 2, 16 * D_ * 2, OWARM);
    stream<16>(RSRC(A.Wo, HE * D_ * 2), gr * (D_ * 2) + p * 16, gr * (D_ * 2) + (p ^ 2) * 16, lb,
      [&](int u, int i) { return (unsigned)(((1024 * w + 64 * u + 16 * i) * D_ + 32 * cs) * 2); },
      [&](auto cc, int so, auto fin) __attribute__((always_inline)) {
      constexpr int c = cc; constexpr bool F = fin;
        if constexpr (!F) {
#pragma unroll
          for (int e = 0; e < 8; ++e) { const v2u f = trd(((e & 4) ? r1 : r0) + so + ((e >> 1) & 1) * 512 + (e & 1) * 256);
            W[c >> 1][16 * (c & 1) + 2 * e] = f.x; W[c >> 1][16 * (c & 1) + 2 * e + 1] = f.y; }
        } else if constexpr (c & 1) PINA(c >> 1, W)
        if constexpr (F && c == 1) {
          pt = get_par(A, rw); cb = comm_base(A) + pt.x * 8192u;
          mss = cb + (cs < 16 ? 8 * 16384 + (cs >> 1) * 1024 : 10 * 16384) + (cs & 1) * 512 + rho * 128;
          if (w == 0) {
            ST64(m0v, rw, msv, mss);
#pragma unroll
            for (int k = 1; k < 4; ++k) ST64(((v2u){~0u, ~0u}), rw, msv, mss + k * SLOT);
          }
        }
        if constexpr (F && c == 9) STF(TD_INIT, tid == 0 ? 0u : OOB, F_OWN + (rho * 18 + cs) * 16);
      });
  }
  PRO(0)
  asm volatile("s_barrier" ::: "memory");
  v8u L, SC;
  L[0] = lane * 16; L[1] = ((n >> 1) & 1) ? 0xEEEE : 0x4444;
  L[2] = (w * 4 + 2 * (g & 1)) * 128 + (16 * (g >> 1) + n) * 4;
  L[3] = rl * 128 + cq * 16;
  L[4] = msv;
  L[5] = (lane & 31) * 32; L[6] = L[7] = 0;
  SC[0] = cb + (rho * 4 + w) * 16384;
  SC[1] = mss;
  SC[2] = cb + 10 * 16384 + 4096 + copy * 1024; SC[3] = pt.y; SC[4] = w == 0 ? 6 : 0; SC[5] = W_TRACE + ((32 + rho * 18 + cs) * 4 + w) * 1024; SC[6] = F_OWN + (rho * 18 + cs) * 16; SC[7] = 0;
#pragma unroll
  for (int i = 0; i < 7; ++i) SC[i] = __builtin_amdgcn_readfirstlane(SC[i]);
  wait_flags<2>(A, rw, (lane & 31) * 8, F_HR, 1);
  PRO(1)
  v4u res;
  asm volatile("s_nop 4\n\t" O_ASM_TEXT : "={v[100:103]}"(res) : AIN(W), "{v[0:7]}"(L), "{s[40:47]}"(SC), "{s[24:27]}"(rw) : O_ASM_CLOB);
  if (res[3]) STF(1, 0, F_TMO);
}

__device__ __forceinline__ void after_owners(const Args& A, rsrc_t rw) {
  if (threadIdx.x < 64) wait_flags<32>(A, rw, 0, F_KV + (blockIdx.x & 31) * 128, AFTK);
  asm volatile("s_barrier" ::: "memory");
}
__global__ void __launch_bounds__(256, 1) k_main(KArgs K) {
  extern __shared__ __attribute__((aligned(16))) char lds[];
  Args A; (KArgs&)A = K;
  auto rw = RSRC(A.wb, W_END);
  A.tag = __builtin_amdgcn_readfirstlane(LD32(rw, 0, F_EPOCH)) + 1u;
  int* sh = (int*)(lds + L_END);
  unsigned long long pc; int clen;
  asm volatile("s_getpc_b64 %0\nkcode_here:\n\ts_mov_b32 %1, kcode_end-kcode_here" : "=s"(pc), "=s"(clen));
  const unsigned cv = __builtin_amdgcn_raw_buffer_load_b32(RSRC(pc, max(clen, 0)), threadIdx.x < 64 ? OOB : ((blockIdx.x >> 3) * 192 + threadIdx.x - 64) * 16, 0, 0);
  if (threadIdx.x == 0) {
    const unsigned long long mark = ((unsigned long long)~A.tag << 32) | A.tag;
    int slot = (xcc_id() & 7u) * 32 + ((blockIdx.x >> 3) & 31), tries = 0;
    while (__hip_atomic_exchange((unsigned long long*)(A.wb + F_SLOT + slot * 64), mark, __ATOMIC_RELAXED, __HIP_MEMORY_SCOPE_AGENT) == mark) {
      slot = (slot + 1) & 255;
      if (++tries == 256) { slot = -1; break; }
    }
    sh[0] = slot;
  }
  __syncthreads();
  const int slot = __builtin_amdgcn_readfirstlane(sh[0]);
  __syncthreads();
  asm volatile("" :: "v"(cv));
  if (slot < 0) return;
  const int x = slot >> 5, r = slot & 31;
  const int w = __builtin_amdgcn_readfirstlane(threadIdx.x >> 6);
  enum { R_HEAD, R_OWN, R_KV, R_Q, R_QA, R_WO, R_F2, R_F1, R_NONE };
  int role, i0 = 0, i1 = 0, job = -1;
  if (x == 1 || x == 3) { if (r < 16) { role = R_HEAD; i0 = r + 16 * (x >> 1); } else { role = R_QA; i0 = r - 16 + 16 * (x >> 1); } }
  else if (x < 4) {
    if (r < 18) { role = R_OWN; i0 = r; i1 = x >> 1; }
    else { role = R_F1; i0 = 8 + 2 * (r - 18) + (x >> 1); }
  } else {
    if (r < 18) { role = x < 6 ? R_WO : R_F2; i0 = r; i1 = x & 1; job = 4 * r + (x - 4); }
    else if (r < 26) { role = R_KV; i0 = (r - 18) + 8 * (x - 4); }
    else if (r < 30) { role = R_Q; i0 = (r - 26) + 4 * (x - 4); }
    else { role = R_F1; i0 = (r - 30) * 4 + (x - 4); }
  }
  {
    const int lane = threadIdx.x & 63;
    const int lj = role == R_QA ? i0 : (role == R_F1 && i0 < 32) ? 32 + i0 : -1;
    if (WARM1 && lj < 0 && job < 60) {
      const int k = x >= 4 ? 68 + (x - 4) * 28 + (r < 15 ? r : r - 3) : (x & 1) ? 36 + r + 16 * (x >> 1) : r + 18 * (x >> 1);
      if (k < 180 && (x < 4 ? r < 18 : r < 30 || true)) {
        const unsigned so = (k * 4 + w) * 1024;
        warm(A.Wk, D_ * HE * 2, lane * 16, so, 720 * 1024, 7); warm(A.Wv, D_ * HE * 2, lane * 16, so, 720 * 1024, 7);
        warm(A.Wq, D_ * HE * 2, lane * 16, so, 720 * 1024, 7);
        if (WARM1 > 1) warm(A.Wo, D_ * HE * 2, lane * 16, so, 720 * 1024, 7);
        VMWAIT0();
      }
    }
    if (lj >= 0) { ln_job(A, rw, lds, lj); ln_job(A, rw, lds, lj + 64); }
    if (job >= 60) calibrate(A, rw, job - 60);
  }
  if (role == R_KV || role == R_Q) {
    const bool isq = role == R_Q;
    if (isq) delay_us(DLY_QA);
    if (isq || w < 2) kv_wave<true>(A, rw, lds, isq ? A.Wq : A.Wk, isq ? i0 * 256 + 64 * w : i0 * 128 + 64 * w, isq ? W_Q + (2 * i0 + (w >> 1)) * 8192 : W_KVX + i0 * 16384,
                                    isq ? H_ * 8192 : H_ * 16384, (isq ? F_Q : F_KV) + 128 * i0, w, isq ? -1 : i0);
    else kv_wave<false>(A, rw, lds, A.Wv, i0 * 128 + 64 * (w & 1), W_KVX + i0 * 16384, H_ * 16384, F_KV + 128 * i0, w, i0);
    role = r > 26 ? R_NONE : (x < 6 ? R_WO : R_F2); i0 = 2 * (r - 18) + (x & 1); i1 = 2;
  }
  if (role == R_HEAD) head_role(A, rw, lds, i0, x >> 1);
  else if (role == R_OWN) own_role(A, rw, lds, i0, i1, 2 + i1);
  else if (role == R_QA) qa_role(A, rw, lds, i0);
  else if (role == R_WO) { if (i1 < 2) delay_us(DLY_WO); t2_role<false>(A, rw, lds, i0, i1); }
  else if (role == R_F2) { if (i1 < 2) after_owners(A, rw); t2_role<true>(A, rw, lds, i0, i1); }
  else if (role == R_F1) { after_owners(A, rw); f1_role(A, rw, lds, i0 >> 1, i0 & 1);
  if (slot == 255) {
    for (unsigned spins = 0; spins < SPINCAP; ++spins) { asm volatile("" ::: "memory"); if (__all(LD32(rw, threadIdx.x * 64, F_SLOT) == A.tag)) break; __builtin_amdgcn_s_sleep(8); }
    asm volatile("s_barrier" ::: "memory");
    if (threadIdx.x == 0) ST32(A.tag, rw, 0, F_EPOCH);
  }
  }
}

extern "C" void kernel_launch(void* const* d_in, const int* in_sizes, int n_in, void* d_out, int out_size, void* d_ws, size_t ws_size, hipStream_t stream) {
  char* wb = (char*)d_ws;
  KArgs A{(const u16*)d_in[0], (const u16*)d_in[1], (const u16*)d_in[2], (const u16*)d_in[3], (const u16*)d_in[4], (const u16*)d_in[5],
         (const u16*)d_in[8], (const u16*)d_in[9], (const u16*)d_in[10], (const u16*)d_in[11], (u16*)d_out, wb};
  (void)hipFuncSetAttribute((const void*)k_main, hipFuncAttributeMaxDynamicSharedMemorySize, LDS_BYTES);
  k_main<<<256, 256, LDS_BYTES, stream>>>(A);
}
```
